```python
import torch

def _softmax(x, axis=-1):
    return torch.softmax(x, dim=axis)
def _gelu(x):
    return torch.nn.functional.gelu(x, approximate="tanh")
def _silu(x):
    return torch.nn.functional.silu(x)
def _sigmoid(x):
    return torch.sigmoid(x)
def _layernorm(x, axis=-1, eps=1e-5):
    m = x.mean(dim=axis, keepdim=True)
    v = x.var(dim=axis, keepdim=True, unbiased=False)
    return (x - m) / torch.sqrt(v + eps)
def _scatter_add(tgt, idx, src, axis=0):
    return tgt.clone().index_add_(axis, idx, src)
def _segment_sum(src, seg, n):
    out = torch.zeros((n,) + src.shape[1:], dtype=src.dtype)
    return out.index_add_(0, seg, src)
def _pad(x, pw):
    flat = tuple(v for pair in reversed(pw) for v in pair)
    return torch.nn.functional.pad(x, flat)

kernel_name = 'standalone__structured_sparse_n_m__ragged'

def setup_inputs():

    _w = (lambda a: (lambda t: t.to(torch.bfloat16) if t.is_floating_point() else t)(torch.as_tensor(a)))
    B, S, D = 4, 2048, 576
    assert D % 4 == 0, "d_model must be divisible by M=4 for 2:4 sparsity"
    return {
        "x": _w((lambda *s: torch.randn(*s, dtype=torch.bfloat16) * 0.05)(B, S, D)),
        "w": _w((lambda *s: torch.randn(*s, dtype=torch.bfloat16) * 0.05)(D, D)),
        "b": _w((lambda *s: torch.randn(*s, dtype=torch.bfloat16) * 0.05)(D)),
    }

def reference(x, w, b):
    B, S, D = x.shape
    N, M = 2, 4
    G = D // M
    wg = w.reshape(G, M, D)
    mag = torch.abs(wg)
    order = torch.argsort(mag, 1)
    thr = (lambda x, i, axis: torch.take_along_dim(x, i, dim=axis))(mag, order[:, M - N:M - N + 1, :], 1)
    mask = (lambda x, dt: x.to(dt))(mag >= thr, w.dtype)
    wp = (wg * mask).reshape(D, D)
    return torch.einsum("bsd,df->bsf", x, wp) + b
```

```cpp
#include <hip/hip_runtime.h>
#include <hip/hip_bf16.h>
#include <stdint.h>

typedef __attribute__((ext_vector_type(8))) short bf16x8;
typedef __attribute__((ext_vector_type(4))) float f32x4;
typedef __attribute__((ext_vector_type(4))) unsigned u32x4;

constexpr int D = 576;
constexpr int MROWS = 8192;

__global__ void prune_kernel(const uint16_t* __restrict__ w, uint16_t* __restrict__ wt) {
    int t = blockIdx.x * blockDim.x + threadIdx.x;
    if (t >= (D / 4) * D) return;
    int n = t % D, g = t / D;
    uint16_t v[4]; unsigned m[4];
    for (int i = 0; i < 4; ++i) { v[i] = w[(g * 4 + i) * D + n]; m[i] = v[i] & 0x7FFF; }
    unsigned h1 = max(m[0], m[1]), l1 = min(m[0], m[1]), h2 = max(m[2], m[3]), l2 = min(m[2], m[3]);
    unsigned thr = max(min(h1, h2), max(l1, l2));
    for (int i = 0; i < 4; ++i) wt[n * D + g * 4 + i] = (m[i] >= thr) ? v[i] : (uint16_t)0;
}

__device__ __forceinline__ unsigned pack_bf16(float a, float b) {
    __hip_bfloat16 x = __float2bfloat16(a), y = __float2bfloat16(b);
    return (unsigned)(*(uint16_t*)&x) | ((unsigned)(*(uint16_t*)&y) << 16);
}

__global__ __launch_bounds__(256) void gemm_kernel(const uint16_t* __restrict__ x, const uint16_t* __restrict__ wt,
                                                   const uint16_t* __restrict__ bias, uint16_t* __restrict__ out) {
    int bid = blockIdx.x;
    int xcd = bid & 7, j = bid >> 3;
    int rb = xcd * 8 + (j >> 2), cb = j & 3;
    int wave = threadIdx.x >> 6, lane = threadIdx.x & 63;
    int r0 = rb * 128 + wave * 32, c0 = cb * 144;
    int fr = lane & 15, fq = lane >> 4;
    f32x4 acc[2][9];
    for (int n = 0; n < 9; ++n) {
        float bv = __bfloat162float(*(const __hip_bfloat16*)&bias[c0 + n * 16 + fr]);
        for (int m = 0; m < 2; ++m) acc[m][n] = (f32x4){bv, bv, bv, bv};
    }
    const uint16_t* xa = x + (size_t)(r0 + fr) * D + fq * 8;
    const uint16_t* wb = wt + (size_t)(c0 + fr) * D + fq * 8;
    for (int k = 0; k < D; k += 32) {
        bf16x8 a[2], b[9];
        for (int m = 0; m < 2; ++m) a[m] = *(const bf16x8*)(xa + m * 16 * D + k);
        for (int n = 0; n < 9; ++n) b[n] = *(const bf16x8*)(wb + n * 16 * D + k);
        for (int m = 0; m < 2; ++m)
            for (int n = 0; n < 9; ++n)
                acc[m][n] = __builtin_amdgcn_mfma_f32_16x16x32_bf16(a[m], b[n], acc[m][n], 0, 0, 0);
    }
    for (int m = 0; m < 2; ++m)
        for (int n = 0; n < 9; ++n)
            for (int jj = 0; jj < 4; ++jj) {
                __hip_bfloat16 o = __float2bfloat16(acc[m][n][jj]);
                out[(size_t)(r0 + m * 16 + fq * 4 + jj) * D + c0 + n * 16 + fr] = *(uint16_t*)&o;
            }
}

extern "C" void kernel_launch(void* const* d_in, const int* in_sizes, int n_in,
                              void* d_out, int out_size, void* d_ws, size_t ws_size,
                              hipStream_t stream) {
    const uint16_t* x = (const uint16_t*)d_in[0];
    const uint16_t* w = (const uint16_t*)d_in[1];
    const uint16_t* b = (const uint16_t*)d_in[2];
    uint16_t* out = (uint16_t*)d_out;
    uint16_t* wt = (uint16_t*)d_ws;
    prune_kernel<<<(D / 4 * D + 255) / 256, 256, 0, stream>>>(w, wt);
    gemm_kernel<<<256, 256, 0, stream>>>(x, wt, b, out);
}
```

```cpp
#include <hip/hip_runtime.h>
#include <stdint.h>

typedef __attribute__((ext_vector_type(8))) short bf16x8;
typedef __attribute__((ext_vector_type(4))) short s16x4;
typedef __attribute__((ext_vector_type(2))) short s16x2;
typedef __attribute__((ext_vector_type(2))) unsigned short u16x2;
typedef __attribute__((ext_vector_type(4))) float f32x4;
typedef __attribute__((ext_vector_type(4))) unsigned u32x4;
typedef __attribute__((ext_vector_type(2))) unsigned u32x2;
typedef __attribute__((ext_vector_type(2))) __bf16 bf16x2_t;
typedef __attribute__((ext_vector_type(2))) float f32x2;
#define LDS3 __attribute__((address_space(3)))
#define DEVI __device__ __forceinline__

#define STORE_AUX 16

constexpr int ROWB = 1152;
constexpr int KB = 1024;
constexpr int BIASB = 4096;

#define ST(i)
template <int N> DEVI void wait_bar() {
    __builtin_amdgcn_sched_barrier(0);
    asm volatile("s_waitcnt vmcnt(%0) lgkmcnt(0)\n\ts_barrier" ::"n"(N) : "memory");
    __builtin_amdgcn_sched_barrier(0);
}
DEVI void lgkm_bar() {
    __builtin_amdgcn_sched_barrier(0);
    asm volatile("s_waitcnt lgkmcnt(0)\n\ts_barrier" ::: "memory");
    __builtin_amdgcn_sched_barrier(0);
}

template <int C, int U> DEVI void dma_set(unsigned wb, unsigned v, u32x4 r, unsigned so) {
    asm volatile("s_add_u32 m0, %0, %1\n\ts_nop 0\n\tbuffer_load_dwordx4 %2, %3, %4 offen offset:%5 lds"
                 ::"s"(wb), "n"(C), "v"(v), "s"(r), "s"(so), "n"(U * 1024) : "memory", "scc");
}
template <int U> DEVI void dma_nxt(unsigned v, u32x4 r, unsigned so) {
    asm volatile("buffer_load_dwordx4 %0, %1, %2 offen offset:%3 lds" ::"v"(v), "s"(r), "s"(so), "n"(U * 1024) : "memory");
}
#define SB() __builtin_amdgcn_sched_barrier(0)
DEVI unsigned opaque_v(unsigned x) { asm volatile("" : "+v"(x)); return x; }
DEVI unsigned opaque_s(unsigned x) { asm volatile("" : "+s"(x)); return x; }

DEVI u32x4 make_srd(const void* p, unsigned bytes) {
    uint64_t a = (uint64_t)p;
    u32x4 r;
    r[0] = __builtin_amdgcn_readfirstlane((unsigned)a);
    r[1] = __builtin_amdgcn_readfirstlane((unsigned)(a >> 32) & 0xffffu);
    r[2] = __builtin_amdgcn_readfirstlane(bytes);
    r[3] = 0x00020000u;
    return r;
}

DEVI void prune4(unsigned d0, unsigned d1, unsigned& r0, unsigned& r1, unsigned kabs, unsigned ksgn) {
    unsigned m0 = d0 & kabs, m1 = d1 & kabs;
    u16x2 a = __builtin_bit_cast(u16x2, m0), b = __builtin_bit_cast(u16x2, m1);
    u16x2 H = __builtin_elementwise_max(a, b), L = __builtin_elementwise_min(a, b);
    u16x2 Hs = {H[1], H[0]}, Ls = {L[1], L[0]};
    u16x2 thr = __builtin_elementwise_max(__builtin_elementwise_min(H, Hs), __builtin_elementwise_max(L, Ls));
    unsigned T = ksgn - __builtin_bit_cast(unsigned, thr);
    unsigned t0 = m0 + T, t1 = m1 + T;
    s16x2 k0 = __builtin_bit_cast(s16x2, t0) >> 15, k1 = __builtin_bit_cast(s16x2, t1) >> 15;
    r0 = d0 & __builtin_bit_cast(unsigned, k0);
    r1 = d1 & __builtin_bit_cast(unsigned, k1);
}

DEVI unsigned cvt_pk(float a, float b) {
    f32x2 v = {a, b};
    return __builtin_bit_cast(unsigned, __builtin_convertvector(v, bf16x2_t));
}

template <bool BT> struct Cfg {
    static constexpr int RA = BT ? 6 : 4;
    static constexpr int MULT = BT ? 4 : 3;
    static constexpr int SLOT = MULT * 8 * KB;
    static constexpr int NB = BT ? 3 : 2;
    static constexpr int BSTEP = BT ? 64 * KB : 72 * KB;
    static constexpr int PBASE = 144 * KB;
    static constexpr int slot(int s) {
        if (BT) { int c = (s + 6) % 5; return c * SLOT; }
        return (s % 6) * SLOT;
    }
    static constexpr int late(int v) {
        if (BT) return (v >> 2) * SLOT + (v & 3) * KB;
        if (v < 2) return slot(8) + v * KB;
        return slot(3 + (v - 2) / 3) + ((v - 2) % 3) * KB;
    }
};

template <bool BT> struct Ctx {
    using C = Cfg<BT>;
    LDS3 char* lds;
    u32x4 srdx, srdw;
    __amdgpu_buffer_rsrc_t srdo;
    unsigned wb;
    unsigned vw0, vw1;
    unsigned vx0, vx1, vx2;
    unsigned vlate;
    unsigned h;
    unsigned kabs, ksgn;
    unsigned wrd[C::NB][2];
    unsigned wrd2[2];
    unsigned xrd[C::NB][2];
    unsigned vout;
    u32x2 wraw[2][4];
    bf16x8 wf[9][2];
    bf16x8 xf[2][6];
    f32x4 acc[C::RA][2];
    f32x4 pacc[2][2][2];
    f32x4 bias[2];
};

template <bool BT, int S, int BUF> DEVI void read_wraw(Ctx<BT>& c) {
    using C = Cfg<BT>;
#pragma unroll
    for (int t = 0; t < 2; ++t)
#pragma unroll
        for (int e = 0; e < 2; ++e) {
            unsigned off;
            if (!BT && S == 0) off = c.wrd2[t] + e * 2 * KB;
            else {
                constexpr int sl = C::slot(S - 1), b = sl / C::BSTEP;
                off = c.wrd[b][t] + (sl - b * C::BSTEP) + e * (BT ? 512 : 3 * KB);
            }
            s16x4 v = __builtin_amdgcn_ds_read_tr16_b64_v4i16((LDS3 s16x4*)(c.lds + off));
            c.wraw[BUF][t * 2 + e] = __builtin_bit_cast(u32x2, v);
        }
}
template <bool BT, int S, int BUF, int T> DEVI void prune_w(Ctx<BT>& c) {
    u32x4 r;
    unsigned a, b;
    prune4(c.wraw[BUF][T * 2][0], c.wraw[BUF][T * 2][1], a, b, c.kabs, c.ksgn); r[0] = a; r[1] = b;
    prune4(c.wraw[BUF][T * 2 + 1][0], c.wraw[BUF][T * 2 + 1][1], a, b, c.kabs, c.ksgn); r[2] = a; r[3] = b;
    c.wf[S][T] = __builtin_bit_cast(bf16x8, r);
}
template <bool BT, int S, int BUF> DEVI void read_xa(Ctx<BT>& c) {
    using C = Cfg<BT>;
    constexpr int sl = C::slot(S), b = sl / C::BSTEP;
#pragma unroll
    for (int tl = 0; tl < C::RA; ++tl) {
        unsigned off = c.xrd[b][tl & 1] + (sl - b * C::BSTEP) + (BT ? (1 + (tl >> 1)) * KB : ((tl >> 1) * 12 + 2) * KB);
        c.xf[BUF][tl] = *(const LDS3 bf16x8*)(c.lds + off);
    }
}
template <bool BT, int S, int BUF, int T0, int T1> DEVI void mma_a(Ctx<BT>& c) {
#pragma unroll
    for (int tl = T0; tl < T1; ++tl)
#pragma unroll
        for (int t = 0; t < 2; ++t)
            c.acc[tl][t] = __builtin_amdgcn_mfma_f32_16x16x32_bf16(c.wf[S][t], c.xf[BUF][tl], S == 0 ? c.bias[t] : c.acc[tl][t], 0, 0, 0);
}
template <bool BT, int P, int S, int IDX, int BUF> DEVI void read_xp1(Ctx<BT>& c) {
    using C = Cfg<BT>;
    constexpr int un = 9 * P + S;
    constexpr int lo = BT ? C::late(S) : C::late(un >> 1);
    constexpr int b = lo / C::BSTEP;
    constexpr int rest = lo - b * C::BSTEP + (BT ? 0 : (un & 1) * 12 * KB);
    c.xf[BUF][IDX * 2 + 0] = *(const LDS3 bf16x8*)(c.lds + c.xrd[b][0] + rest);
    c.xf[BUF][IDX * 2 + 1] = *(const LDS3 bf16x8*)(c.lds + c.xrd[b][1] + rest);
}
template <bool BT, int P, int B, int BUF> DEVI void read_xp(Ctx<BT>& c) {
    read_xp1<BT, P, 3 * B, 0, BUF>(c); read_xp1<BT, P, 3 * B + 1, 1, BUF>(c); read_xp1<BT, P, 3 * B + 2, 2, BUF>(c);
}
template <bool BT, int P, int B, int BUF, int I0 = 0, int I1 = 3> DEVI void mma_p(Ctx<BT>& c) {
#pragma unroll
    for (int i = I0; i < I1; ++i)
#pragma unroll
        for (int par = 0; par < 2; ++par)
#pragma unroll
            for (int t = 0; t < 2; ++t)
                c.pacc[P & 1][par][t] = __builtin_amdgcn_mfma_f32_16x16x32_bf16(c.wf[3 * B + i][t], c.xf[BUF][i * 2 + par],
                                                                                (B == 0 && i == 0) ? c.bias[t] : c.pacc[P & 1][par][t], 0, 0, 0);
}
template <bool BT> DEVI void ex_write(Ctx<BT>& c, unsigned a0, unsigned a1, const f32x4& v0, const f32x4& v1) {
    *(LDS3 f32x4*)(c.lds + a0) = v0;
    *(LDS3 f32x4*)(c.lds + a1) = v1;
}
struct Ex { f32x4 p0, p1; };
template <bool BT> DEVI Ex ex_read(Ctx<BT>& c, unsigned a0, unsigned a1) {
    Ex e;
    e.p0 = *(const LDS3 f32x4*)(c.lds + a0);
    e.p1 = *(const LDS3 f32x4*)(c.lds + a1);
    return e;
}
DEVI float addf(float a, float b) { float r; asm("v_add_f32 %0, %1, %2" : "=v"(r) : "v"(a), "v"(b)); return r; }
template <bool BT, int TL> DEVI void finalize(Ctx<BT>& c, const Ex& e, const f32x4& v0, const f32x4& v1) {
    f32x4 s0 = v0 + e.p0, s1 = v1 + e.p1;
    unsigned q00 = cvt_pk(s0[0], s0[1]), q01 = cvt_pk(s0[2], s0[3]), q10 = cvt_pk(s1[0], s1[1]), q11 = cvt_pk(s1[2], s1[3]);
    auto x0 = __builtin_amdgcn_permlane16_swap(q00, q10, false, false);
    auto x1 = __builtin_amdgcn_permlane16_swap(q01, q11, false, false);
    u32x4 o = {x0[0], x1[0], x0[1], x1[1]};
    __builtin_amdgcn_raw_buffer_store_b128(o, c.srdo, c.vout, TL * (BT ? 32 : 16) * ROWB, STORE_AUX);
}

template <bool BT, int V> DEVI unsigned late_soff(Ctx<BT>& c) {
    constexpr int u = V < 2 ? V : (V - 2) % 3;
    constexpr int u0 = 2 * V, u1 = 2 * V + 1;
    constexpr int S0 = (64 + 32 * (u0 / 9)) * ROWB + (u0 % 9) * 128 - u * KB;
    constexpr int S1 = (64 + 32 * (u1 / 9)) * ROWB + (u1 % 9) * 128 - u * KB;
    return S0 + c.h * (S1 - S0);
}

template <bool BT, int N> DEVI void issue(Ctx<BT>& c) {
    using C = Cfg<BT>;
    constexpr int WCH = 64 * ROWB;
    if constexpr (BT) {
        if constexpr (N == 0) dma_set<C::slot(-1), 0>(c.wb, c.vw0, c.srdw, 0);
        else if constexpr (N <= 32) {
            constexpr int s = (N - 1) / 4, u = (N - 1) % 4;
            if constexpr (u == 0) dma_set<C::slot(s), 0>(c.wb, c.vw0, c.srdw, (s + 1) * WCH);
            else dma_nxt<u>(u == 1 ? c.vx0 : (u == 2 ? c.vx1 : c.vx2), c.srdx, s * 128);
        } else if constexpr (N == 33) dma_set<C::slot(8), 1>(c.wb, c.vx0, c.srdx, 8 * 128);
        else if constexpr (N <= 35) dma_nxt<N - 32>(N == 34 ? c.vx1 : c.vx2, c.srdx, 8 * 128);
        else if constexpr (N <= 44) {
            constexpr int v = N - 36, so = 192 * ROWB + v * 128 - (v & 3) * KB;
            if constexpr ((v & 3) == 0) dma_set<C::late(v), 0>(c.wb, c.vlate, c.srdx, so);
            else dma_nxt<(v & 3)>(c.vlate, c.srdx, so);
        }
    } else {
        if constexpr (N == 0) dma_set<C::PBASE, 0>(c.wb - (c.wb / 3072) * 1024, c.vw0, c.srdw, 0);
        else if constexpr (N == 1) dma_nxt<1>(c.vw1, c.srdw, 0);
        else if constexpr (N <= 25) {
            constexpr int s = (N - 2) / 3, u = (N - 2) % 3;
            if constexpr (u == 0) dma_set<C::slot(s), 0>(c.wb, c.vw0, c.srdw, (s + 1) * WCH);
            else if constexpr (u == 1) dma_nxt<1>(c.vw1, c.srdw, (s + 1) * WCH);
            else dma_nxt<2>(c.vx0, c.srdx, s * 128);
        } else if constexpr (N == 26) dma_set<C::slot(8), 0>(c.wb, c.vlate, c.srdx, late_soff<BT, 0>(c));
        else if constexpr (N == 27) dma_nxt<1>(c.vlate, c.srdx, late_soff<BT, 1>(c));
        else if constexpr (N == 28) dma_nxt<2>(c.vx0, c.srdx, 8 * 128);
        else if constexpr (N <= 40) {
            constexpr int v = N - 27, u = (v - 2) % 3;
            if constexpr (u == 0) dma_set<C::late(v), 0>(c.wb, c.vlate, c.srdx, late_soff<BT, v>(c));
            else dma_nxt<u>(c.vlate, c.srdx, late_soff<BT, v>(c));
        }
    }
}
template <bool BT, int N0, int N1> DEVI void issue_range(Ctx<BT>& c) {
    if constexpr (N0 < N1) { issue<BT, N0>(c); issue_range<BT, N0 + 1, N1>(c); }
}
template <bool BT> constexpr int issued(int s) {
    if (BT) { constexpr int t[11] = {9, 13, 17, 21, 25, 29, 33, 36, 40, 44, 45}; return t[s + 1]; }
    else    { constexpr int t[11] = {8, 11, 15, 19, 23, 26, 29, 32, 35, 38, 41}; return t[s + 1]; }
}
template <bool BT> constexpr int need(int s) {
    if (BT) return s < 0 ? 1 : (s < 8 ? 1 + 4 * (s + 1) : 36);
    return s < 0 ? 2 : 2 + 3 * (s + 1);
}

template <bool BT, int S> DEVI void stage_a(Ctx<BT>& c) {
    using C = Cfg<BT>;
    constexpr int N0 = issued<BT>(S), N1 = issued<BT>(S + 1);
    constexpr int H = C::RA / 2;
    ST(3 + 2 * S);
    wait_bar<N0 - need<BT>(S)>();
    ST(4 + 2 * S);
    if constexpr (S < 8) read_wraw<BT, S + 1, (S + 1) & 1>(c);
    read_xa<BT, S, S & 1>(c);
    SB();
    if constexpr (N0 < N1) issue<BT, N0>(c);
    SB();
    prune_w<BT, S, S & 1, 0>(c);
    if constexpr (S > 0) mma_a<BT, S - 1, (S - 1) & 1, 0, 1>(c);
    SB();
    if constexpr (N0 + 1 < N1) issue<BT, N0 + 1>(c);
    SB();
    if constexpr (S > 0) mma_a<BT, S - 1, (S - 1) & 1, 1, H>(c);
    SB();
    if constexpr (N0 + 2 < N1) issue<BT, N0 + 2>(c);
    SB();
    prune_w<BT, S, S & 1, 1>(c);
    if constexpr (S > 0) mma_a<BT, S - 1, (S - 1) & 1, H, H + 1>(c);
    SB();
    if constexpr (N0 + 3 < N1) issue<BT, N0 + 3>(c);
    SB();
    if constexpr (S > 0) mma_a<BT, S - 1, (S - 1) & 1, H + 1, C::RA>(c);
}

template <bool BT> DEVI void body(const uint16_t* x, const uint16_t* w, const uint16_t* bias, uint16_t* out,
                                  LDS3 char* lds, int row0, int nrows, int col0, unsigned* wsp) {
    using C = Cfg<BT>;
    Ctx<BT> c;
    c.lds = lds;
    ST(0);
    const int lane = threadIdx.x & 63;
    const int wv = __builtin_amdgcn_readfirstlane(threadIdx.x >> 6);
    const int h = wv >> 2, cg = wv & 3;
    const int cc = BT ? (cg & 1) : cg, sg = BT ? (cg >> 1) : 0;
    c.h = h;
    c.srdx = make_srd((const char*)x + (size_t)row0 * ROWB - BIASB, nrows * ROWB + BIASB);
    c.srdw = make_srd((const char*)w - BIASB, 576 * ROWB + BIASB);
    c.srdo = __builtin_amdgcn_make_buffer_rsrc((void*)(out + (size_t)row0 * 576), 0, nrows * ROWB, 0x00020000);
    c.wb = wv * C::MULT * KB;
    const int xlane = (lane >> 3) * ROWB + (((lane & 7) ^ (4 * (wv & 1) + (lane >> 4))) * 16) + BIASB;
    if constexpr (BT) {
        const int q1 = (lane >> 3) & 3, g = (q1 >> 1) | ((wv & 1) << 1);
        c.vw0 = (8 * wv + (lane >> 3)) * ROWB + col0 * 2 + (((lane & 7) ^ (g << 1)) * 16) + BIASB;
        c.vw1 = 0;
        c.vx0 = 8 * wv * ROWB + xlane - KB;
        c.vx1 = c.vx0 + 64 * ROWB - KB;
        c.vx2 = c.vx1 + 64 * ROWB - KB;
        c.vlate = 8 * wv * ROWB + xlane;
    } else {
        const int g = (lane >> 4) | (((wv >> 1) & 1) << 2);
        c.vw0 = (4 * wv + (lane >> 4)) * ROWB + col0 * 2 + (((lane & 15) ^ (g << 1)) * 16) + BIASB;
        c.vw1 = c.vw0 + 32 * ROWB - KB;
        c.vx0 = 8 * wv * ROWB + xlane - 2 * KB;
        c.vx1 = c.vx2 = 0;
        c.vlate = 8 * (wv & 3) * ROWB + xlane;
    }
    issue_range<BT, 0, need<BT>(0)>(c);
    const int q = lane >> 4, gm = lane & 15;
    u32x2 braw[2];
    {
        const uint16_t* bp = bias + col0 + 32 * cc + 4 * q;
        asm volatile("global_load_dwordx2 %0, %2, off\n\tglobal_load_dwordx2 %1, %2, off offset:32"
                     : "=&v"(braw[0]), "=&v"(braw[1]) : "v"(bp) : "memory");
    }
    issue_range<BT, need<BT>(0), issued<BT>(-1)>(c);
    unsigned exw, exr;
    c.kabs = opaque_s(0x7fff7fffu);
    c.ksgn = opaque_s(0x80008000u);
    {
        const int q1 = gm >> 2, p = gm & 3;
#pragma unroll
        for (int t = 0; t < 2; ++t) {
            unsigned o;
            if constexpr (BT) {
                const int g = (q1 >> 1) | ((q & 1) << 1);
                o = 4 * (4 * h + q) * KB + q1 * 128 + (((2 * cc + t) ^ g) * 32) + 8 * p;
                c.wrd2[t] = 0;
            } else {
                const int g = q1 | ((q & 1) << 2);
                const int in = q1 * 256 + (((2 * cc + t) ^ g) * 32) + 8 * p;
                o = (6 * q + h) * KB + in;
                c.wrd2[t] = C::PBASE + (4 * q + h) * KB + in;
            }
#pragma unroll
            for (int b = 0; b < C::NB; ++b) c.wrd[b][t] = opaque_v(o + b * C::BSTEP);
        }
        const int j = gm;
        const int in = (j & 7) * 128 + (((4 * h + q) ^ (j >> 1)) * 16);
#pragma unroll
        for (int par = 0; par < 2; ++par) {
            unsigned o;
            if constexpr (BT) o = 4 * KB * (4 * (par ^ h) + 2 * sg + (j >> 3)) + in;
            else o = 3 * KB * (2 * (par ^ h) + (j >> 3)) + in;
#pragma unroll
            for (int b = 0; b < C::NB; ++b) c.xrd[b][par] = opaque_v(o + b * C::BSTEP);
        }
        exw = wv * 2 * KB + lane * 16;
        exr = (wv ^ 4) * 2 * KB + lane * 16;
        const int trow = BT ? (32 * h + 16 * sg + j) : (16 * h + j);
        c.vout = trow * ROWB + (col0 + 32 * cc + 16 * (q & 1) + 8 * (q >> 1)) * 2;
    }
    ST(1);
    wait_bar<issued<BT>(-1) + 2 - need<BT>(-1)>();
    ST(2);
    read_wraw<BT, 0, 0>(c);
    SB();
    issue_range<BT, issued<BT>(-1), issued<BT>(0)>(c);
    SB();
    asm volatile("s_waitcnt vmcnt(%2)" : "+v"(braw[0]), "+v"(braw[1]) : "n"(issued<BT>(0) - need<BT>(0)) : "memory");
    SB();
#pragma unroll
    for (int t = 0; t < 2; ++t) {
        unsigned b0 = h ? 0u : braw[t][0], b1 = h ? 0u : braw[t][1];
        c.bias[t][0] = __builtin_bit_cast(float, b0 << 16);
        c.bias[t][1] = __builtin_bit_cast(float, b0 & 0xffff0000u);
        c.bias[t][2] = __builtin_bit_cast(float, b1 << 16);
        c.bias[t][3] = __builtin_bit_cast(float, b1 & 0xffff0000u);
    }
    stage_a<BT, 0>(c); stage_a<BT, 1>(c); stage_a<BT, 2>(c); stage_a<BT, 3>(c); stage_a<BT, 4>(c);
    stage_a<BT, 5>(c); stage_a<BT, 6>(c); stage_a<BT, 7>(c); stage_a<BT, 8>(c);

    if constexpr (BT) {
        constexpr int EA = 3 * C::SLOT;
        constexpr int EP = 3 * C::SLOT + 48 * KB;
        const unsigned w0 = opaque_v(exw + EA), r0 = opaque_v(exr + EA);
        wait_bar<3>();
        read_xp<BT, 0, 0, 1>(c);
        SB();
        mma_a<BT, 8, 0, 0, 6>(c);
        SB();
#pragma unroll
        for (int m = 0; m < 3; ++m) ex_write(c, w0 + m * 16 * KB, w0 + m * 16 * KB + KB, c.acc[2 * m + 1][0], c.acc[2 * m + 1][1]);
        read_xp<BT, 0, 1, 0>(c);
        SB();
        mma_p<BT, 0, 0, 1>(c);
        wait_bar<0>();
        Ex e0 = ex_read(c, r0, r0 + KB), e1 = ex_read(c, r0 + 16 * KB, r0 + 17 * KB), e2 = ex_read(c, r0 + 32 * KB, r0 + 33 * KB);
        read_xp<BT, 0, 2, 1>(c);
        SB();
        mma_p<BT, 0, 1, 0, 0, 1>(c);
        SB();
        finalize<BT, 0>(c, e0, c.acc[0][0], c.acc[0][1]);
        finalize<BT, 2>(c, e1, c.acc[2][0], c.acc[2][1]);
        finalize<BT, 4>(c, e2, c.acc[4][0], c.acc[4][1]);
        mma_p<BT, 0, 1, 0, 1, 3>(c);
        SB();
        mma_p<BT, 0, 2, 1>(c);
        ex_write(c, w0 + 48 * KB, w0 + 49 * KB, c.pacc[0][1][0], c.pacc[0][1][1]);
        lgkm_bar();
        Ex e3 = ex_read(c, r0 + 48 * KB, r0 + 49 * KB);
        finalize<BT, 6>(c, e3, c.pacc[0][0][0], c.pacc[0][0][1]);
        (void)EP;
    } else {
        const unsigned w0 = opaque_v(exw + C::PBASE), r0 = opaque_v(exr + C::PBASE);
        const unsigned w1 = opaque_v(exw + C::slot(1)), r1 = opaque_v(exr + C::slot(1));
        const unsigned e2w0 = C::slot(1) + 16 * KB + wv * KB + lane * 16, e2w1 = C::slot(2) + (3 * wv + 2) * KB + lane * 16;
        const unsigned e2r0 = C::slot(1) + 16 * KB + (wv ^ 4) * KB + lane * 16, e2r1 = C::slot(2) + (3 * (wv ^ 4) + 2) * KB + lane * 16;
        ST(21); wait_bar<11>(); ST(22);
        read_xp<BT, 0, 0, 1>(c);
        SB();
        mma_a<BT, 8, 0, 0, 4>(c);
        SB();
        ex_write(c, w0, w0 + KB, c.acc[1][0], c.acc[1][1]);
        ex_write(c, w1, w1 + KB, c.acc[3][0], c.acc[3][1]);
        read_xp<BT, 0, 1, 0>(c);
        SB();
        mma_p<BT, 0, 0, 1>(c);
        ST(23); wait_bar<9>(); ST(24);
        Ex ea = ex_read(c, r0, r0 + KB), eb = ex_read(c, r1, r1 + KB);
        read_xp<BT, 0, 2, 1>(c);
        SB();
        mma_p<BT, 0, 1, 0, 0, 1>(c);
        SB();
        finalize<BT, 0>(c, ea, c.acc[0][0], c.acc[0][1]);
        finalize<BT, 2>(c, eb, c.acc[2][0], c.acc[2][1]);
        mma_p<BT, 0, 1, 0, 1, 3>(c);
        ST(25); wait_bar<8>(); ST(26);
        read_xp<BT, 1, 0, 0>(c);
        SB();
        mma_p<BT, 0, 2, 1>(c);
        SB();
        read_xp<BT, 1, 1, 1>(c);
        SB();
        mma_p<BT, 1, 0, 0, 0, 1>(c);
        SB();
        ex_write(c, e2w0, e2w1, c.pacc[0][1][0], c.pacc[0][1][1]);
        SB();
        mma_p<BT, 1, 0, 0, 1, 3>(c);
        ST(27); wait_bar<7>(); ST(28);
        ea = ex_read(c, e2r0, e2r1);
        read_xp<BT, 1, 2, 0>(c);
        SB();
        mma_p<BT, 1, 1, 1, 0, 1>(c);
        SB();
        finalize<BT, 4>(c, ea, c.pacc[0][0][0], c.pacc[0][0][1]);
        mma_p<BT, 1, 1, 1, 1, 3>(c);
        if (nrows > 128) {
            ST(29); wait_bar<5>(); ST(30);
            read_xp<BT, 2, 0, 1>(c);
            SB();
            mma_p<BT, 1, 2, 0>(c);
            SB();
            read_xp<BT, 2, 1, 0>(c);
            SB();
            mma_p<BT, 2, 0, 1, 0, 1>(c);
            SB();
            ex_write(c, w0, w0 + KB, c.pacc[1][1][0], c.pacc[1][1][1]);
            SB();
            mma_p<BT, 2, 0, 1, 1, 3>(c);
            ST(31); wait_bar<3>(); ST(32);
            ea = ex_read(c, r0, r0 + KB);
            read_xp<BT, 2, 2, 1>(c);
            SB();
            mma_p<BT, 2, 1, 0, 0, 1>(c);
            SB();
            finalize<BT, 6>(c, ea, c.pacc[1][0][0], c.pacc[1][0][1]);
            mma_p<BT, 2, 1, 0, 1, 3>(c);
            SB();
            mma_p<BT, 2, 2, 1>(c);
            ex_write(c, w1, w1 + KB, c.pacc[0][1][0], c.pacc[0][1][1]);
            ST(33); lgkm_bar(); ST(34);
            ea = ex_read(c, r1, r1 + KB);
            finalize<BT, 8>(c, ea, c.pacc[0][0][0], c.pacc[0][0][1]);
        } else {
            SB();
            mma_p<BT, 1, 2, 0>(c);
            ex_write(c, w0, w0 + KB, c.pacc[1][1][0], c.pacc[1][1][1]);
            lgkm_bar();
            ea = ex_read(c, r0, r0 + KB);
            finalize<BT, 6>(c, ea, c.pacc[1][0][0], c.pacc[1][0][1]);
            asm volatile("s_waitcnt vmcnt(0)" ::: "memory");
        }
    }
}

__global__ __launch_bounds__(512, 2) void sparse24_gemm_kernel(const uint16_t* __restrict__ x, const uint16_t* __restrict__ w,
                                                               const uint16_t* __restrict__ bias, uint16_t* __restrict__ out, unsigned* wsp) {
    __shared__ __attribute__((aligned(1024))) char smem[163840];
    LDS3 char* lds = (LDS3 char*)smem;
    const int bid = blockIdx.x, xcd = bid & 7, j = bid >> 3;
    if (j < 28) {
        const int rbl = j >> 2, cb = j & 3;
        const int r0 = rbl < 4 ? rbl * 160 : 640 + (rbl - 4) * 128;
        body<false>(x, w, bias, out, lds, xcd * 1024 + r0, rbl < 4 ? 160 : 128, cb * 128, wsp);
    } else {
        body<true>(x, w, bias, out, lds, xcd * 1024 + (j - 28) * 256, 256, 512, wsp);
    }
}

extern "C" void kernel_launch(void* const* d_in, const int* in_sizes, int n_in, void* d_out, int out_size, void* d_ws,
                              size_t ws_size, hipStream_t stream) {
    sparse24_gemm_kernel<<<256, 512, 0, stream>>>((const uint16_t*)d_in[0], (const uint16_t*)d_in[1], (const uint16_t*)d_in[2],
                                                  (uint16_t*)d_out, (unsigned*)d_ws);
}
```

```cpp
#define CODEPF 15872
#include <hip/hip_runtime.h>
#include <stdint.h>

typedef __attribute__((ext_vector_type(8))) short bf16x8;
typedef __attribute__((ext_vector_type(4))) short s16x4;
typedef __attribute__((ext_vector_type(2))) short s16x2;
typedef __attribute__((ext_vector_type(2))) unsigned short u16x2;
typedef __attribute__((ext_vector_type(4))) float f32x4;
typedef __attribute__((ext_vector_type(4))) unsigned u32x4;
typedef __attribute__((ext_vector_type(2))) unsigned u32x2;
typedef __attribute__((ext_vector_type(2))) __bf16 bf16x2_t;
typedef __attribute__((ext_vector_type(2))) float f32x2;
#define LDS3 __attribute__((address_space(3)))
#define DEVI __device__ __forceinline__

#define STORE_AUX 16

constexpr int ROWB = 1152;
constexpr int KB = 1024;
constexpr int BIASB = 4096;

#define ST(i)
template <int N> DEVI void wait_bar() {
    __builtin_amdgcn_sched_barrier(0);
    asm volatile("s_waitcnt vmcnt(%0) lgkmcnt(0)\n\ts_barrier" ::"n"(N) : "memory");
    __builtin_amdgcn_sched_barrier(0);
}
DEVI void lgkm_bar() {
    __builtin_amdgcn_sched_barrier(0);
    asm volatile("s_waitcnt lgkmcnt(0)\n\ts_barrier" ::: "memory");
    __builtin_amdgcn_sched_barrier(0);
}

#define XPOL ""
#define WPOL ""
struct SrdX { u32x4 r; };
struct SrdW { u32x4 r; };
template <int C, int U> DEVI void dma_set(unsigned wb, unsigned v, SrdX r, unsigned so) {
    asm volatile("s_add_u32 m0, %0, %1\n\ts_nop 0\n\tbuffer_load_dwordx4 %2, %3, %4 offen offset:%5" XPOL " lds"
                 ::"s"(wb), "n"(C), "v"(v), "s"(r.r), "s"(so), "n"(U * 1024) : "memory", "scc");
}
template <int U> DEVI void dma_nxt(unsigned v, SrdX r, unsigned so) {
    asm volatile("buffer_load_dwordx4 %0, %1, %2 offen offset:%3" XPOL " lds" ::"v"(v), "s"(r.r), "s"(so), "n"(U * 1024) : "memory");
}
template <int C, int U> DEVI void dma_set(unsigned wb, unsigned v, SrdW r, unsigned so) {
    asm volatile("s_add_u32 m0, %0, %1\n\ts_nop 0\n\tbuffer_load_dwordx4 %2, %3, %4 offen offset:%5" WPOL " lds"
                 ::"s"(wb), "n"(C), "v"(v), "s"(r.r), "s"(so), "n"(U * 1024) : "memory", "scc");
}
template <int U> DEVI void dma_nxt(unsigned v, SrdW r, unsigned so) {
    asm volatile("buffer_load_dwordx4 %0, %1, %2 offen offset:%3" WPOL " lds" ::"v"(v), "s"(r.r), "s"(so), "n"(U * 1024) : "memory");
}
#define SB() __builtin_amdgcn_sched_barrier(0)
DEVI unsigned opaque_v(unsigned x) { asm volatile("" : "+v"(x)); return x; }
DEVI unsigned opaque_s(unsigned x) { asm volatile("" : "+s"(x)); return x; }

DEVI u32x4 make_srd(const void* p, unsigned bytes) {
    uint64_t a = (uint64_t)p;
    u32x4 r;
    r[0] = __builtin_amdgcn_readfirstlane((unsigned)a);
    r[1] = __builtin_amdgcn_readfirstlane((unsigned)(a >> 32) & 0xffffu);
    r[2] = __builtin_amdgcn_readfirstlane(bytes);
    r[3] = 0x00020000u;
    return r;
}

DEVI void prune4(unsigned d0, unsigned d1, unsigned& r0, unsigned& r1, unsigned kabs, unsigned ksgn) {
    unsigned m0 = d0 & kabs, m1 = d1 & kabs;
    u16x2 a = __builtin_bit_cast(u16x2, m0), b = __builtin_bit_cast(u16x2, m1);
    u16x2 H = __builtin_elementwise_max(a, b), L = __builtin_elementwise_min(a, b);
    u16x2 Hs = {H[1], H[0]}, Ls = {L[1], L[0]};
    u16x2 thr = __builtin_elementwise_max(__builtin_elementwise_min(H, Hs), __builtin_elementwise_max(L, Ls));
    unsigned T = ksgn - __builtin_bit_cast(unsigned, thr);
    unsigned t0 = m0 + T, t1 = m1 + T;
    s16x2 k0 = __builtin_bit_cast(s16x2, t0) >> 15, k1 = __builtin_bit_cast(s16x2, t1) >> 15;
    r0 = d0 & __builtin_bit_cast(unsigned, k0);
    r1 = d1 & __builtin_bit_cast(unsigned, k1);
}

DEVI unsigned cvt_pk(float a, float b) {
    f32x2 v = {a, b};
    return __builtin_bit_cast(unsigned, __builtin_convertvector(v, bf16x2_t));
}

template <bool BT> struct Cfg {
    static constexpr int RA = BT ? 6 : 4;
    static constexpr int MULT = BT ? 4 : 3;
    static constexpr int SLOT = MULT * 8 * KB;
    static constexpr int NB = BT ? 3 : 2;
    static constexpr int BSTEP = BT ? 64 * KB : 72 * KB;
    static constexpr int PBASE = 144 * KB;
    static constexpr int slot(int s) {
        if (BT) { int c = (s + 6) % 5; return c * SLOT; }
        return (s % 6) * SLOT;
    }
    static constexpr int late(int v) {
        if (BT) return (v >> 2) * SLOT + (v & 3) * KB;
        if (v < 2) return slot(8) + v * KB;
        return slot(3 + (v - 2) / 3) + ((v - 2) % 3) * KB;
    }
};

template <bool BT> struct Ctx {
    using C = Cfg<BT>;
    LDS3 char* lds;
    SrdX srdx; SrdW srdw;
    __amdgpu_buffer_rsrc_t srdo;
    unsigned wb;
    unsigned vw0, vw1;
    unsigned vx0, vx1, vx2;
    unsigned vlate;
    unsigned h;
    unsigned kabs, ksgn;
    unsigned wrd[C::NB][2];
    unsigned wrd2[2];
    unsigned xrd[C::NB][2];
    unsigned vout;
    u32x2 wraw[2][4];
    bf16x8 wf[9][2];
    bf16x8 xf[2][6];
    f32x4 acc[C::RA][2];
    f32x4 pacc[2][2][2];
    f32x4 bias[2];
};

template <bool BT, int S, int BUF> DEVI void read_wraw(Ctx<BT>& c) {
    using C = Cfg<BT>;
#pragma unroll
    for (int t = 0; t < 2; ++t)
#pragma unroll
        for (int e = 0; e < 2; ++e) {
            unsigned off;
            if (!BT && S == 0) off = c.wrd2[t] + e * 2 * KB;
            else {
                constexpr int sl = C::slot(S - 1), b = sl / C::BSTEP;
                off = c.wrd[b][t] + (sl - b * C::BSTEP) + e * (BT ? 512 : 3 * KB);
            }
            s16x4 v = __builtin_amdgcn_ds_read_tr16_b64_v4i16((LDS3 s16x4*)(c.lds + off));
            c.wraw[BUF][t * 2 + e] = __builtin_bit_cast(u32x2, v);
        }
}
template <bool BT, int S, int BUF, int T> DEVI void prune_w(Ctx<BT>& c) {
    u32x4 r;
    unsigned a, b;
    prune4(c.wraw[BUF][T * 2][0], c.wraw[BUF][T * 2][1], a, b, c.kabs, c.ksgn); r[0] = a; r[1] = b;
    prune4(c.wraw[BUF][T * 2 + 1][0], c.wraw[BUF][T * 2 + 1][1], a, b, c.kabs, c.ksgn); r[2] = a; r[3] = b;
    c.wf[S][T] = __builtin_bit_cast(bf16x8, r);
}
template <bool BT, int S, int BUF> DEVI void read_xa(Ctx<BT>& c) {
    using C = Cfg<BT>;
    constexpr int sl = C::slot(S), b = sl / C::BSTEP;
#pragma unroll
    for (int tl = 0; tl < C::RA; ++tl) {
        unsigned off = c.xrd[b][tl & 1] + (sl - b * C::BSTEP) + (BT ? (1 + (tl >> 1)) * KB : ((tl >> 1) * 12 + 2) * KB);
        c.xf[BUF][tl] = *(const LDS3 bf16x8*)(c.lds + off);
    }
}
template <bool BT, int S, int BUF, int T0, int T1> DEVI void mma_a(Ctx<BT>& c) {
#pragma unroll
    for (int tl = T0; tl < T1; ++tl)
#pragma unroll
        for (int t = 0; t < 2; ++t)
            c.acc[tl][t] = __builtin_amdgcn_mfma_f32_16x16x32_bf16(c.wf[S][t], c.xf[BUF][tl], S == 0 ? c.bias[t] : c.acc[tl][t], 0, 0, 0);
}
template <bool BT, int P, int S, int IDX, int BUF> DEVI void read_xp1(Ctx<BT>& c) {
    using C = Cfg<BT>;
    constexpr int un = 9 * P + S;
    constexpr int lo = BT ? C::late(S) : C::late(un >> 1);
    constexpr int b = lo / C::BSTEP;
    constexpr int rest = lo - b * C::BSTEP + (BT ? 0 : (un & 1) * 12 * KB);
    c.xf[BUF][IDX * 2 + 0] = *(const LDS3 bf16x8*)(c.lds + c.xrd[b][0] + rest);
    c.xf[BUF][IDX * 2 + 1] = *(const LDS3 bf16x8*)(c.lds + c.xrd[b][1] + rest);
}
template <bool BT, int P, int B, int BUF> DEVI void read_xp(Ctx<BT>& c) {
    read_xp1<BT, P, 3 * B, 0, BUF>(c); read_xp1<BT, P, 3 * B + 1, 1, BUF>(c); read_xp1<BT, P, 3 * B + 2, 2, BUF>(c);
}
template <bool BT, int P, int B, int BUF, int I0 = 0, int I1 = 3> DEVI void mma_p(Ctx<BT>& c) {
#pragma unroll
    for (int i = I0; i < I1; ++i)
#pragma unroll
        for (int par = 0; par < 2; ++par)
#pragma unroll
            for (int t = 0; t < 2; ++t)
                c.pacc[P & 1][par][t] = __builtin_amdgcn_mfma_f32_16x16x32_bf16(c.wf[3 * B + i][t], c.xf[BUF][i * 2 + par],
                                                                                (B == 0 && i == 0) ? c.bias[t] : c.pacc[P & 1][par][t], 0, 0, 0);
}
template <bool BT> DEVI void ex_write(Ctx<BT>& c, unsigned a0, unsigned a1, const f32x4& v0, const f32x4& v1) {
    *(LDS3 f32x4*)(c.lds + a0) = v0;
    *(LDS3 f32x4*)(c.lds + a1) = v1;
}
struct Ex { f32x4 p0, p1; };
template <bool BT> DEVI Ex ex_read(Ctx<BT>& c, unsigned a0, unsigned a1) {
    Ex e;
    e.p0 = *(const LDS3 f32x4*)(c.lds + a0);
    e.p1 = *(const LDS3 f32x4*)(c.lds + a1);
    return e;
}
DEVI float addf(float a, float b) { float r; asm("v_add_f32 %0, %1, %2" : "=v"(r) : "v"(a), "v"(b)); return r; }
template <bool BT, int TL> DEVI void finalize(Ctx<BT>& c, const Ex& e, const f32x4& v0, const f32x4& v1) {
    f32x4 s0 = v0 + e.p0, s1 = v1 + e.p1;
    unsigned q00 = cvt_pk(s0[0], s0[1]), q01 = cvt_pk(s0[2], s0[3]), q10 = cvt_pk(s1[0], s1[1]), q11 = cvt_pk(s1[2], s1[3]);
    auto x0 = __builtin_amdgcn_permlane16_swap(q00, q10, false, false);
    auto x1 = __builtin_amdgcn_permlane16_swap(q01, q11, false, false);
    u32x4 o = {x0[0], x1[0], x0[1], x1[1]};
    __builtin_amdgcn_raw_buffer_store_b128(o, c.srdo, c.vout, TL * (BT ? 32 : 16) * ROWB, STORE_AUX);
}

template <bool BT, int V> DEVI unsigned late_soff(Ctx<BT>& c) {
    constexpr int u = V < 2 ? V : (V - 2) % 3;
    constexpr int u0 = 2 * V, u1 = 2 * V + 1;
    constexpr int S0 = (64 + 32 * (u0 / 9)) * ROWB + (u0 % 9) * 128 - u * KB;
    constexpr int S1 = (64 + 32 * (u1 / 9)) * ROWB + (u1 % 9) * 128 - u * KB;
    return S0 + c.h * (S1 - S0);
}

template <bool BT, int N> DEVI void issue(Ctx<BT>& c) {
    using C = Cfg<BT>;
    constexpr int WCH = 64 * ROWB;
    if constexpr (BT) {
        if constexpr (N == 0) dma_set<C::slot(-1), 0>(c.wb, c.vw0, c.srdw, 0);
        else if constexpr (N <= 32) {
            constexpr int s = (N - 1) / 4, u = (N - 1) % 4;
            if constexpr (u == 0) dma_set<C::slot(s), 0>(c.wb, c.vw0, c.srdw, (s + 1) * WCH);
            else dma_nxt<u>(u == 1 ? c.vx0 : (u == 2 ? c.vx1 : c.vx2), c.srdx, s * 128);
        } else if constexpr (N == 33) dma_set<C::slot(8), 1>(c.wb, c.vx0, c.srdx, 8 * 128);
        else if constexpr (N <= 35) dma_nxt<N - 32>(N == 34 ? c.vx1 : c.vx2, c.srdx, 8 * 128);
        else if constexpr (N <= 44) {
            constexpr int v = N - 36, so = 192 * ROWB + v * 128 - (v & 3) * KB;
            if constexpr ((v & 3) == 0) dma_set<C::late(v), 0>(c.wb, c.vlate, c.srdx, so);
            else dma_nxt<(v & 3)>(c.vlate, c.srdx, so);
        }
    } else {
        if constexpr (N == 0) dma_set<C::PBASE, 0>(c.wb - (c.wb / 3072) * 1024, c.vw0, c.srdw, 0);
        else if constexpr (N == 1) dma_nxt<1>(c.vw1, c.srdw, 0);
        else if constexpr (N <= 25) {
            constexpr int s = (N - 2) / 3, u = (N - 2) % 3;
            if constexpr (u == 0) dma_set<C::slot(s), 0>(c.wb, c.vw0, c.srdw, (s + 1) * WCH);
            else if constexpr (u == 1) dma_nxt<1>(c.vw1, c.srdw, (s + 1) * WCH);
            else dma_nxt<2>(c.vx0, c.srdx, s * 128);
        } else if constexpr (N == 26) dma_set<C::slot(8), 0>(c.wb, c.vlate, c.srdx, late_soff<BT, 0>(c));
        else if constexpr (N == 27) dma_nxt<1>(c.vlate, c.srdx, late_soff<BT, 1>(c));
        else if constexpr (N == 28) dma_nxt<2>(c.vx0, c.srdx, 8 * 128);
        else if constexpr (N <= 40) {
            constexpr int v = N - 27, u = (v - 2) % 3;
            if constexpr (u == 0) dma_set<C::late(v), 0>(c.wb, c.vlate, c.srdx, late_soff<BT, v>(c));
            else dma_nxt<u>(c.vlate, c.srdx, late_soff<BT, v>(c));
        }
    }
}
template <bool BT, int N0, int N1> DEVI void issue_range(Ctx<BT>& c) {
    if constexpr (N0 < N1) { issue<BT, N0>(c); issue_range<BT, N0 + 1, N1>(c); }
}
template <bool BT> constexpr int issued(int s) {
    if (BT) { constexpr int t[11] = {9, 13, 17, 21, 25, 29, 33, 36, 40, 44, 45}; return t[s + 1]; }
    else    { constexpr int t[11] = {8, 11, 15, 19, 23, 26, 29, 32, 35, 38, 41}; return t[s + 1]; }
}
template <bool BT> constexpr int need(int s) {
    if (BT) return s < 0 ? 1 : (s < 8 ? 1 + 4 * (s + 1) : 36);
    return s < 0 ? 2 : 2 + 3 * (s + 1);
}

template <bool BT, int S> DEVI void stage_a(Ctx<BT>& c) {
    using C = Cfg<BT>;
    constexpr int N0 = issued<BT>(S), N1 = issued<BT>(S + 1);
    constexpr int H = C::RA / 2;
    ST(3 + 2 * S);
    wait_bar<N0 - need<BT>(S)>();
    ST(4 + 2 * S);
    if constexpr (S < 8) read_wraw<BT, S + 1, (S + 1) & 1>(c);
    read_xa<BT, S, S & 1>(c);
    SB();
    if constexpr (N0 < N1) issue<BT, N0>(c);
    SB();
    prune_w<BT, S, S & 1, 0>(c);
    if constexpr (S > 0) mma_a<BT, S - 1, (S - 1) & 1, 0, 1>(c);
    SB();
    if constexpr (N0 + 1 < N1) issue<BT, N0 + 1>(c);
    SB();
    if constexpr (S > 0) mma_a<BT, S - 1, (S - 1) & 1, 1, H>(c);
    SB();
    if constexpr (N0 + 2 < N1) issue<BT, N0 + 2>(c);
    SB();
    prune_w<BT, S, S & 1, 1>(c);
    if constexpr (S > 0) mma_a<BT, S - 1, (S - 1) & 1, H, H + 1>(c);
    SB();
    if constexpr (N0 + 3 < N1) issue<BT, N0 + 3>(c);
    SB();
    if constexpr (S > 0) mma_a<BT, S - 1, (S - 1) & 1, H + 1, C::RA>(c);
}

template <bool BT> DEVI void body(const uint16_t* x, const uint16_t* w, const uint16_t* bias, uint16_t* out,
                                  LDS3 char* lds, int row0, int nrows, int col0, unsigned* wsp) {
    using C = Cfg<BT>;
    Ctx<BT> c;
    c.lds = lds;
    ST(0);
    const int lane = threadIdx.x & 63;
    const int wv = __builtin_amdgcn_readfirstlane(threadIdx.x >> 6);
    const int h = wv >> 2, cg = wv & 3;
    const int cc = BT ? (cg & 1) : cg, sg = BT ? (cg >> 1) : 0;
    c.h = h;
    c.srdx.r = make_srd((const char*)x + (size_t)row0 * ROWB - BIASB, nrows * ROWB + BIASB);
    c.srdw.r = make_srd((const char*)w - BIASB, 576 * ROWB + BIASB);
    c.srdo = __builtin_amdgcn_make_buffer_rsrc((void*)(out + (size_t)row0 * 576), 0, nrows * ROWB, 0x00020000);
    c.wb = wv * C::MULT * KB;
    const int xlane = (lane >> 3) * ROWB + (((lane & 7) ^ (4 * (wv & 1) + (lane >> 4))) * 16) + BIASB;
    if constexpr (BT) {
        const int q1 = (lane >> 3) & 3, g = (q1 >> 1) | ((wv & 1) << 1);
        c.vw0 = (8 * wv + (lane >> 3)) * ROWB + col0 * 2 + (((lane & 7) ^ (g << 1)) * 16) + BIASB;
        c.vw1 = 0;
        c.vx0 = 8 * wv * ROWB + xlane - KB;
        c.vx1 = c.vx0 + 64 * ROWB - KB;
        c.vx2 = c.vx1 + 64 * ROWB - KB;
        c.vlate = 8 * wv * ROWB + xlane;
    } else {
        const int g = (lane >> 4) | (((wv >> 1) & 1) << 2);
        c.vw0 = (4 * wv + (lane >> 4)) * ROWB + col0 * 2 + (((lane & 15) ^ (g << 1)) * 16) + BIASB;
        c.vw1 = c.vw0 + 32 * ROWB - KB;
        c.vx0 = 8 * wv * ROWB + xlane - 2 * KB;
        c.vx1 = c.vx2 = 0;
        c.vlate = 8 * (wv & 3) * ROWB + xlane;
    }
    issue_range<BT, 0, need<BT>(0)>(c);
    const int q = lane >> 4, gm = lane & 15;
    u32x2 braw[2];
    {
        const uint16_t* bp = bias + col0 + 32 * cc + 4 * q;
        asm volatile("global_load_dwordx2 %0, %2, off\n\tglobal_load_dwordx2 %1, %2, off offset:32"
                     : "=&v"(braw[0]), "=&v"(braw[1]) : "v"(bp) : "memory");
    }
    issue_range<BT, need<BT>(0), issued<BT>(-1)>(c);
    unsigned exw, exr;
    c.kabs = opaque_s(0x7fff7fffu);
    c.ksgn = opaque_s(0x80008000u);
    {
        const int q1 = gm >> 2, p = gm & 3;
#pragma unroll
        for (int t = 0; t < 2; ++t) {
            unsigned o;
            if constexpr (BT) {
                const int g = (q1 >> 1) | ((q & 1) << 1);
                o = 4 * (4 * h + q) * KB + q1 * 128 + (((2 * cc + t) ^ g) * 32) + 8 * p;
                c.wrd2[t] = 0;
            } else {
                const int g = q1 | ((q & 1) << 2);
                const int in = q1 * 256 + (((2 * cc + t) ^ g) * 32) + 8 * p;
                o = (6 * q + h) * KB + in;
                c.wrd2[t] = C::PBASE + (4 * q + h) * KB + in;
            }
#pragma unroll
            for (int b = 0; b < C::NB; ++b) c.wrd[b][t] = opaque_v(o + b * C::BSTEP);
        }
        const int j = gm;
        const int in = (j & 7) * 128 + (((4 * h + q) ^ (j >> 1)) * 16);
#pragma unroll
        for (int par = 0; par < 2; ++par) {
            unsigned o;
            if constexpr (BT) o = 4 * KB * (4 * (par ^ h) + 2 * sg + (j >> 3)) + in;
            else o = 3 * KB * (2 * (par ^ h) + (j >> 3)) + in;
#pragma unroll
            for (int b = 0; b < C::NB; ++b) c.xrd[b][par] = opaque_v(o + b * C::BSTEP);
        }
        exw = wv * 2 * KB + lane * 16;
        exr = (wv ^ 4) * 2 * KB + lane * 16;
        const int trow = BT ? (32 * h + 16 * sg + j) : (16 * h + j);
        c.vout = trow * ROWB + (col0 + 32 * cc + 16 * (q & 1) + 8 * (q >> 1)) * 2;
    }
    ST(1);
    wait_bar<issued<BT>(-1) + 2 - need<BT>(-1)>();
    ST(2);
    read_wraw<BT, 0, 0>(c);
    SB();
    issue_range<BT, issued<BT>(-1), issued<BT>(0)>(c);
    SB();
    asm volatile("s_waitcnt vmcnt(%2)" : "+v"(braw[0]), "+v"(braw[1]) : "n"(issued<BT>(0) - need<BT>(0)) : "memory");
    SB();
#pragma unroll
    for (int t = 0; t < 2; ++t) {
        unsigned b0 = h ? 0u : braw[t][0], b1 = h ? 0u : braw[t][1];
        c.bias[t][0] = __builtin_bit_cast(float, b0 << 16);
        c.bias[t][1] = __builtin_bit_cast(float, b0 & 0xffff0000u);
        c.bias[t][2] = __builtin_bit_cast(float, b1 << 16);
        c.bias[t][3] = __builtin_bit_cast(float, b1 & 0xffff0000u);
    }
    stage_a<BT, 0>(c); stage_a<BT, 1>(c); stage_a<BT, 2>(c); stage_a<BT, 3>(c); stage_a<BT, 4>(c);
    stage_a<BT, 5>(c); stage_a<BT, 6>(c); stage_a<BT, 7>(c); stage_a<BT, 8>(c);

    if constexpr (BT) {
        constexpr int EA = 3 * C::SLOT;
        constexpr int EP = 3 * C::SLOT + 48 * KB;
        const unsigned w0 = opaque_v(exw + EA), r0 = opaque_v(exr + EA);
        wait_bar<3>();
        read_xp<BT, 0, 0, 1>(c);
        SB();
        mma_a<BT, 8, 0, 0, 6>(c);
        SB();
#pragma unroll
        for (int m = 0; m < 3; ++m) ex_write(c, w0 + m * 16 * KB, w0 + m * 16 * KB + KB, c.acc[2 * m + 1][0], c.acc[2 * m + 1][1]);
        read_xp<BT, 0, 1, 0>(c);
        SB();
        mma_p<BT, 0, 0, 1>(c);
        wait_bar<0>();
        Ex e0 = ex_read(c, r0, r0 + KB), e1 = ex_read(c, r0 + 16 * KB, r0 + 17 * KB), e2 = ex_read(c, r0 + 32 * KB, r0 + 33 * KB);
        read_xp<BT, 0, 2, 1>(c);
        SB();
        mma_p<BT, 0, 1, 0, 0, 1>(c);
        SB();
        finalize<BT, 0>(c, e0, c.acc[0][0], c.acc[0][1]);
        finalize<BT, 2>(c, e1, c.acc[2][0], c.acc[2][1]);
        finalize<BT, 4>(c, e2, c.acc[4][0], c.acc[4][1]);
        mma_p<BT, 0, 1, 0, 1, 3>(c);
        SB();
        mma_p<BT, 0, 2, 1>(c);
        ex_write(c, w0 + 48 * KB, w0 + 49 * KB, c.pacc[0][1][0], c.pacc[0][1][1]);
        lgkm_bar();
        Ex e3 = ex_read(c, r0 + 48 * KB, r0 + 49 * KB);
        finalize<BT, 6>(c, e3, c.pacc[0][0][0], c.pacc[0][0][1]);
        (void)EP;
    } else {
        const unsigned w0 = opaque_v(exw + C::PBASE), r0 = opaque_v(exr + C::PBASE);
        const unsigned w1 = opaque_v(exw + C::slot(1)), r1 = opaque_v(exr + C::slot(1));
        const unsigned e2w0 = C::slot(1) + 16 * KB + wv * KB + lane * 16, e2w1 = C::slot(2) + (3 * wv + 2) * KB + lane * 16;
        const unsigned e2r0 = C::slot(1) + 16 * KB + (wv ^ 4) * KB + lane * 16, e2r1 = C::slot(2) + (3 * (wv ^ 4) + 2) * KB + lane * 16;
        ST(21); wait_bar<11>(); ST(22);
        read_xp<BT, 0, 0, 1>(c);
        SB();
        mma_a<BT, 8, 0, 0, 4>(c);
        SB();
        ex_write(c, w0, w0 + KB, c.acc[1][0], c.acc[1][1]);
        ex_write(c, w1, w1 + KB, c.acc[3][0], c.acc[3][1]);
        read_xp<BT, 0, 1, 0>(c);
        SB();
        mma_p<BT, 0, 0, 1>(c);
        ST(23); wait_bar<9>(); ST(24);
        Ex ea = ex_read(c, r0, r0 + KB), eb = ex_read(c, r1, r1 + KB);
        read_xp<BT, 0, 2, 1>(c);
        SB();
        mma_p<BT, 0, 1, 0, 0, 1>(c);
        SB();
        finalize<BT, 0>(c, ea, c.acc[0][0], c.acc[0][1]);
        finalize<BT, 2>(c, eb, c.acc[2][0], c.acc[2][1]);
        mma_p<BT, 0, 1, 0, 1, 3>(c);
        ST(25); wait_bar<8>(); ST(26);
        read_xp<BT, 1, 0, 0>(c);
        SB();
        mma_p<BT, 0, 2, 1>(c);
        SB();
        read_xp<BT, 1, 1, 1>(c);
        SB();
        mma_p<BT, 1, 0, 0, 0, 1>(c);
        SB();
        ex_write(c, e2w0, e2w1, c.pacc[0][1][0], c.pacc[0][1][1]);
        SB();
        mma_p<BT, 1, 0, 0, 1, 3>(c);
        ST(27); wait_bar<7>(); ST(28);
        ea = ex_read(c, e2r0, e2r1);
        read_xp<BT, 1, 2, 0>(c);
        SB();
        mma_p<BT, 1, 1, 1, 0, 1>(c);
        SB();
        finalize<BT, 4>(c, ea, c.pacc[0][0][0], c.pacc[0][0][1]);
        mma_p<BT, 1, 1, 1, 1, 3>(c);
        if (nrows > 128) {
            ST(29); wait_bar<5>(); ST(30);
            read_xp<BT, 2, 0, 1>(c);
            SB();
            mma_p<BT, 1, 2, 0>(c);
            SB();
            read_xp<BT, 2, 1, 0>(c);
            SB();
            mma_p<BT, 2, 0, 1, 0, 1>(c);
            SB();
            ex_write(c, w0, w0 + KB, c.pacc[1][1][0], c.pacc[1][1][1]);
            SB();
            mma_p<BT, 2, 0, 1, 1, 3>(c);
            ST(31); wait_bar<3>(); ST(32);
            ea = ex_read(c, r0, r0 + KB);
            read_xp<BT, 2, 2, 1>(c);
            SB();
            mma_p<BT, 2, 1, 0, 0, 1>(c);
            SB();
            finalize<BT, 6>(c, ea, c.pacc[1][0][0], c.pacc[1][0][1]);
            mma_p<BT, 2, 1, 0, 1, 3>(c);
            SB();
            mma_p<BT, 2, 2, 1>(c);
            ex_write(c, w1, w1 + KB, c.pacc[0][1][0], c.pacc[0][1][1]);
            ST(33); lgkm_bar(); ST(34);
            ea = ex_read(c, r1, r1 + KB);
            finalize<BT, 8>(c, ea, c.pacc[0][0][0], c.pacc[0][0][1]);
        } else {
            SB();
            mma_p<BT, 1, 2, 0>(c);
            ex_write(c, w0, w0 + KB, c.pacc[1][1][0], c.pacc[1][1][1]);
            lgkm_bar();
            ea = ex_read(c, r0, r0 + KB);
            finalize<BT, 6>(c, ea, c.pacc[1][0][0], c.pacc[1][0][1]);
            asm volatile("s_waitcnt vmcnt(0)" ::: "memory");
        }
    }
}

__global__ __launch_bounds__(512, 2) void sparse24_gemm_kernel(const uint16_t* __restrict__ x, const uint16_t* __restrict__ w,
                                                               const uint16_t* __restrict__ bias, uint16_t* __restrict__ out, unsigned* wsp) {
    __shared__ __attribute__((aligned(1024))) char smem[163840];
    LDS3 char* lds = (LDS3 char*)smem;
    {
        uint64_t pc; asm volatile("s_getpc_b64 %0" : "=s"(pc));
        const char* cp = (const char*)(pc & ~(uint64_t)255) + min((int)threadIdx.x * 32, CODEPF - 32);
        unsigned m = __builtin_amdgcn_readfirstlane(136 * KB + (threadIdx.x >> 6) * 256);
        asm volatile("s_mov_b32 m0, %0\n\ts_nop 0\n\tglobal_load_lds_dword %1, off" :: "s"(m), "v"(cp) : "memory");
    }
    const int bid = blockIdx.x, xcd = bid & 7, j = bid >> 3;
    if (j < 28) {
        const int rbl = j >> 2, cb = j & 3;
        const int r0 = rbl < 4 ? rbl * 160 : 640 + (rbl - 4) * 128;
        body<false>(x, w, bias, out, lds, xcd * 1024 + r0, rbl < 4 ? 160 : 128, cb * 128, wsp);
    } else {
        body<true>(x, w, bias, out, lds, xcd * 1024 + (j - 28) * 256, 256, 512, wsp);
    }
}

extern "C" void kernel_launch(void* const* d_in, const int* in_sizes, int n_in, void* d_out, int out_size, void* d_ws,
                              size_t ws_size, hipStream_t stream) {
    sparse24_gemm_kernel<<<256, 512, 0, stream>>>((const uint16_t*)d_in[0], (const uint16_t*)d_in[1], (const uint16_t*)d_in[2],
                                                  (uint16_t*)d_out, (unsigned*)d_ws);
}
```

```cpp
#define TOUCH 4096
#define CODEPF 15872
#include <hip/hip_runtime.h>
#include <stdint.h>

typedef __attribute__((ext_vector_type(8))) short bf16x8;
typedef __attribute__((ext_vector_type(4))) short s16x4;
typedef __attribute__((ext_vector_type(2))) short s16x2;
typedef __attribute__((ext_vector_type(2))) unsigned short u16x2;
typedef __attribute__((ext_vector_type(4))) float f32x4;
typedef __attribute__((ext_vector_type(4))) unsigned u32x4;
typedef __attribute__((ext_vector_type(2))) unsigned u32x2;
typedef __attribute__((ext_vector_type(2))) __bf16 bf16x2_t;
typedef __attribute__((ext_vector_type(2))) float f32x2;
#define LDS3 __attribute__((address_space(3)))
#define DEVI __device__ __forceinline__

#define STORE_AUX 16

constexpr int ROWB = 1152;
constexpr int KB = 1024;
constexpr int BIASB = 4096;

#define ST(i)
template <int N> DEVI void wait_bar() {
    __builtin_amdgcn_sched_barrier(0);
    asm volatile("s_waitcnt vmcnt(%0) lgkmcnt(0)\n\ts_barrier" ::"n"(N) : "memory");
    __builtin_amdgcn_sched_barrier(0);
}
DEVI void lgkm_bar() {
    __builtin_amdgcn_sched_barrier(0);
    asm volatile("s_waitcnt lgkmcnt(0)\n\ts_barrier" ::: "memory");
    __builtin_amdgcn_sched_barrier(0);
}

#define XPOL ""
#define WPOL ""
struct SrdX { u32x4 r; };
struct SrdW { u32x4 r; };
template <int C, int U> DEVI void dma_set(unsigned wb, unsigned v, SrdX r, unsigned so) {
    asm volatile("s_add_u32 m0, %0, %1\n\ts_nop 0\n\tbuffer_load_dwordx4 %2, %3, %4 offen offset:%5" XPOL " lds"
                 ::"s"(wb), "n"(C), "v"(v), "s"(r.r), "s"(so), "n"(U * 1024) : "memory", "scc");
}
template <int U> DEVI void dma_nxt(unsigned v, SrdX r, unsigned so) {
    asm volatile("buffer_load_dwordx4 %0, %1, %2 offen offset:%3" XPOL " lds" ::"v"(v), "s"(r.r), "s"(so), "n"(U * 1024) : "memory");
}
template <int C, int U> DEVI void dma_set(unsigned wb, unsigned v, SrdW r, unsigned so) {
    asm volatile("s_add_u32 m0, %0, %1\n\ts_nop 0\n\tbuffer_load_dwordx4 %2, %3, %4 offen offset:%5" WPOL " lds"
                 ::"s"(wb), "n"(C), "v"(v), "s"(r.r), "s"(so), "n"(U * 1024) : "memory", "scc");
}
template <int U> DEVI void dma_nxt(unsigned v, SrdW r, unsigned so) {
    asm volatile("buffer_load_dwordx4 %0, %1, %2 offen offset:%3" WPOL " lds" ::"v"(v), "s"(r.r), "s"(so), "n"(U * 1024) : "memory");
}
#define SB() __builtin_amdgcn_sched_barrier(0)
DEVI unsigned opaque_v(unsigned x) { asm volatile("" : "+v"(x)); return x; }
DEVI unsigned opaque_s(unsigned x) { asm volatile("" : "+s"(x)); return x; }

DEVI u32x4 make_srd(const void* p, unsigned bytes) {
    uint64_t a = (uint64_t)p;
    u32x4 r;
    r[0] = __builtin_amdgcn_readfirstlane((unsigned)a);
    r[1] = __builtin_amdgcn_readfirstlane((unsigned)(a >> 32) & 0xffffu);
    r[2] = __builtin_amdgcn_readfirstlane(bytes);
    r[3] = 0x00020000u;
    return r;
}

DEVI void prune4(unsigned d0, unsigned d1, unsigned& r0, unsigned& r1, unsigned kabs, unsigned ksgn) {
    unsigned m0 = d0 & kabs, m1 = d1 & kabs;
    u16x2 a = __builtin_bit_cast(u16x2, m0), b = __builtin_bit_cast(u16x2, m1);
    u16x2 H = __builtin_elementwise_max(a, b), L = __builtin_elementwise_min(a, b);
    u16x2 Hs = {H[1], H[0]}, Ls = {L[1], L[0]};
    u16x2 thr = __builtin_elementwise_max(__builtin_elementwise_min(H, Hs), __builtin_elementwise_max(L, Ls));
    unsigned T = ksgn - __builtin_bit_cast(unsigned, thr);
    unsigned t0 = m0 + T, t1 = m1 + T;
    s16x2 k0 = __builtin_bit_cast(s16x2, t0) >> 15, k1 = __builtin_bit_cast(s16x2, t1) >> 15;
    r0 = d0 & __builtin_bit_cast(unsigned, k0);
    r1 = d1 & __builtin_bit_cast(unsigned, k1);
}

DEVI unsigned cvt_pk(float a, float b) {
    f32x2 v = {a, b};
    return __builtin_bit_cast(unsigned, __builtin_convertvector(v, bf16x2_t));
}

template <bool BT> struct Cfg {
    static constexpr int RA = BT ? 6 : 4;
    static constexpr int MULT = BT ? 4 : 3;
    static constexpr int SLOT = MULT * 8 * KB;
    static constexpr int NB = BT ? 3 : 2;
    static constexpr int BSTEP = BT ? 64 * KB : 72 * KB;
    static constexpr int PBASE = 144 * KB;
    static constexpr int slot(int s) {
        if (BT) { int c = (s + 6) % 5; return c * SLOT; }
        return (s % 6) * SLOT;
    }
    static constexpr int late(int v) {
        if (BT) return (v >> 2) * SLOT + (v & 3) * KB;
        if (v < 2) return slot(8) + v * KB;
        return slot(3 + (v - 2) / 3) + ((v - 2) % 3) * KB;
    }
};

template <bool BT> struct Ctx {
    using C = Cfg<BT>;
    LDS3 char* lds;
    SrdX srdx; SrdW srdw;
    __amdgpu_buffer_rsrc_t srdo;
    unsigned wb;
    unsigned vw0, vw1;
    unsigned vx0, vx1, vx2;
    unsigned vlate;
    unsigned h;
    unsigned kabs, ksgn;
    unsigned wrd[C::NB][2];
    unsigned wrd2[2];
    unsigned xrd[C::NB][2];
    unsigned vout;
    u32x2 wraw[2][4];
    bf16x8 wf[9][2];
    bf16x8 xf[2][6];
    f32x4 acc[C::RA][2];
    f32x4 pacc[2][2][2];
    f32x4 bias[2];
};

template <bool BT, int S, int BUF> DEVI void read_wraw(Ctx<BT>& c) {
    using C = Cfg<BT>;
#pragma unroll
    for (int t = 0; t < 2; ++t)
#pragma unroll
        for (int e = 0; e < 2; ++e) {
            unsigned off;
            if (!BT && S == 0) off = c.wrd2[t] + e * 2 * KB;
            else {
                constexpr int sl = C::slot(S - 1), b = sl / C::BSTEP;
                off = c.wrd[b][t] + (sl - b * C::BSTEP) + e * (BT ? 512 : 3 * KB);
            }
            s16x4 v = __builtin_amdgcn_ds_read_tr16_b64_v4i16((LDS3 s16x4*)(c.lds + off));
            c.wraw[BUF][t * 2 + e] = __builtin_bit_cast(u32x2, v);
        }
}
template <bool BT, int S, int BUF, int T> DEVI void prune_w(Ctx<BT>& c) {
    u32x4 r;
    unsigned a, b;
    prune4(c.wraw[BUF][T * 2][0], c.wraw[BUF][T * 2][1], a, b, c.kabs, c.ksgn); r[0] = a; r[1] = b;
    prune4(c.wraw[BUF][T * 2 + 1][0], c.wraw[BUF][T * 2 + 1][1], a, b, c.kabs, c.ksgn); r[2] = a; r[3] = b;
    c.wf[S][T] = __builtin_bit_cast(bf16x8, r);
}
template <bool BT, int S, int BUF> DEVI void read_xa(Ctx<BT>& c) {
    using C = Cfg<BT>;
    constexpr int sl = C::slot(S), b = sl / C::BSTEP;
#pragma unroll
    for (int tl = 0; tl < C::RA; ++tl) {
        unsigned off = c.xrd[b][tl & 1] + (sl - b * C::BSTEP) + (BT ? (1 + (tl >> 1)) * KB : ((tl >> 1) * 12 + 2) * KB);
        c.xf[BUF][tl] = *(const LDS3 bf16x8*)(c.lds + off);
    }
}
template <bool BT, int S, int BUF, int T0, int T1> DEVI void mma_a(Ctx<BT>& c) {
#pragma unroll
    for (int tl = T0; tl < T1; ++tl)
#pragma unroll
        for (int t = 0; t < 2; ++t)
            c.acc[tl][t] = __builtin_amdgcn_mfma_f32_16x16x32_bf16(c.wf[S][t], c.xf[BUF][tl], S == 0 ? c.bias[t] : c.acc[tl][t], 0, 0, 0);
}
template <bool BT, int P, int S, int IDX, int BUF> DEVI void read_xp1(Ctx<BT>& c) {
    using C = Cfg<BT>;
    constexpr int un = 9 * P + S;
    constexpr int lo = BT ? C::late(S) : C::late(un >> 1);
    constexpr int b = lo / C::BSTEP;
    constexpr int rest = lo - b * C::BSTEP + (BT ? 0 : (un & 1) * 12 * KB);
    c.xf[BUF][IDX * 2 + 0] = *(const LDS3 bf16x8*)(c.lds + c.xrd[b][0] + rest);
    c.xf[BUF][IDX * 2 + 1] = *(const LDS3 bf16x8*)(c.lds + c.xrd[b][1] + rest);
}
template <bool BT, int P, int B, int BUF> DEVI void read_xp(Ctx<BT>& c) {
    read_xp1<BT, P, 3 * B, 0, BUF>(c); read_xp1<BT, P, 3 * B + 1, 1, BUF>(c); read_xp1<BT, P, 3 * B + 2, 2, BUF>(c);
}
template <bool BT, int P, int B, int BUF, int I0 = 0, int I1 = 3> DEVI void mma_p(Ctx<BT>& c) {
#pragma unroll
    for (int i = I0; i < I1; ++i)
#pragma unroll
        for (int par = 0; par < 2; ++par)
#pragma unroll
            for (int t = 0; t < 2; ++t)
                c.pacc[P & 1][par][t] = __builtin_amdgcn_mfma_f32_16x16x32_bf16(c.wf[3 * B + i][t], c.xf[BUF][i * 2 + par],
                                                                                (B == 0 && i == 0) ? c.bias[t] : c.pacc[P & 1][par][t], 0, 0, 0);
}
template <bool BT> DEVI void ex_write(Ctx<BT>& c, unsigned a0, unsigned a1, const f32x4& v0, const f32x4& v1) {
    *(LDS3 f32x4*)(c.lds + a0) = v0;
    *(LDS3 f32x4*)(c.lds + a1) = v1;
}
struct Ex { f32x4 p0, p1; };
template <bool BT> DEVI Ex ex_read(Ctx<BT>& c, unsigned a0, unsigned a1) {
    Ex e;
    e.p0 = *(const LDS3 f32x4*)(c.lds + a0);
    e.p1 = *(const LDS3 f32x4*)(c.lds + a1);
    return e;
}
DEVI float addf(float a, float b) { float r; asm("v_add_f32 %0, %1, %2" : "=v"(r) : "v"(a), "v"(b)); return r; }
template <bool BT, int TL> DEVI void finalize(Ctx<BT>& c, const Ex& e, const f32x4& v0, const f32x4& v1) {
    f32x4 s0 = v0 + e.p0, s1 = v1 + e.p1;
    unsigned q00 = cvt_pk(s0[0], s0[1]), q01 = cvt_pk(s0[2], s0[3]), q10 = cvt_pk(s1[0], s1[1]), q11 = cvt_pk(s1[2], s1[3]);
    auto x0 = __builtin_amdgcn_permlane16_swap(q00, q10, false, false);
    auto x1 = __builtin_amdgcn_permlane16_swap(q01, q11, false, false);
    u32x4 o = {x0[0], x1[0], x0[1], x1[1]};
    __builtin_amdgcn_raw_buffer_store_b128(o, c.srdo, c.vout, TL * (BT ? 32 : 16) * ROWB, STORE_AUX);
}

template <bool BT, int V> DEVI unsigned late_soff(Ctx<BT>& c) {
    constexpr int u = V < 2 ? V : (V - 2) % 3;
    constexpr int u0 = 2 * V, u1 = 2 * V + 1;
    constexpr int S0 = (64 + 32 * (u0 / 9)) * ROWB + (u0 % 9) * 128 - u * KB;
    constexpr int S1 = (64 + 32 * (u1 / 9)) * ROWB + (u1 % 9) * 128 - u * KB;
    return S0 + c.h * (S1 - S0);
}

template <bool BT, int N> DEVI void issue(Ctx<BT>& c) {
    using C = Cfg<BT>;
    constexpr int WCH = 64 * ROWB;
    if constexpr (BT) {
        if constexpr (N == 0) dma_set<C::slot(-1), 0>(c.wb, c.vw0, c.srdw, 0);
        else if constexpr (N <= 32) {
            constexpr int s = (N - 1) / 4, u = (N - 1) % 4;
            if constexpr (u == 0) dma_set<C::slot(s), 0>(c.wb, c.vw0, c.srdw, (s + 1) * WCH);
            else dma_nxt<u>(u == 1 ? c.vx0 : (u == 2 ? c.vx1 : c.vx2), c.srdx, s * 128);
        } else if constexpr (N == 33) dma_set<C::slot(8), 1>(c.wb, c.vx0, c.srdx, 8 * 128);
        else if constexpr (N <= 35) dma_nxt<N - 32>(N == 34 ? c.vx1 : c.vx2, c.srdx, 8 * 128);
        else if constexpr (N <= 44) {
            constexpr int v = N - 36, so = 192 * ROWB + v * 128 - (v & 3) * KB;
            if constexpr ((v & 3) == 0) dma_set<C::late(v), 0>(c.wb, c.vlate, c.srdx, so);
            else dma_nxt<(v & 3)>(c.vlate, c.srdx, so);
        }
    } else {
        if constexpr (N == 0) dma_set<C::PBASE, 0>(c.wb - (c.wb / 3072) * 1024, c.vw0, c.srdw, 0);
        else if constexpr (N == 1) dma_nxt<1>(c.vw1, c.srdw, 0);
        else if constexpr (N <= 25) {
            constexpr int s = (N - 2) / 3, u = (N - 2) % 3;
            if constexpr (u == 0) dma_set<C::slot(s), 0>(c.wb, c.vw0, c.srdw, (s + 1) * WCH);
            else if constexpr (u == 1) dma_nxt<1>(c.vw1, c.srdw, (s + 1) * WCH);
            else dma_nxt<2>(c.vx0, c.srdx, s * 128);
        } else if constexpr (N == 26) dma_set<C::slot(8), 0>(c.wb, c.vlate, c.srdx, late_soff<BT, 0>(c));
        else if constexpr (N == 27) dma_nxt<1>(c.vlate, c.srdx, late_soff<BT, 1>(c));
        else if constexpr (N == 28) dma_nxt<2>(c.vx0, c.srdx, 8 * 128);
        else if constexpr (N <= 40) {
            constexpr int v = N - 27, u = (v - 2) % 3;
            if constexpr (u == 0) dma_set<C::late(v), 0>(c.wb, c.vlate, c.srdx, late_soff<BT, v>(c));
            else dma_nxt<u>(c.vlate, c.srdx, late_soff<BT, v>(c));
        }
    }
}
template <bool BT, int N0, int N1> DEVI void issue_range(Ctx<BT>& c) {
    if constexpr (N0 < N1) { issue<BT, N0>(c); issue_range<BT, N0 + 1, N1>(c); }
}
template <bool BT> constexpr int issued(int s) {
    if (BT) { constexpr int t[11] = {9, 13, 17, 21, 25, 29, 33, 36, 40, 44, 45}; return t[s + 1]; }
    else    { constexpr int t[11] = {8, 11, 15, 19, 23, 26, 29, 32, 35, 38, 41}; return t[s + 1]; }
}
template <bool BT> constexpr int need(int s) {
    if (BT) return s < 0 ? 1 : (s < 8 ? 1 + 4 * (s + 1) : 36);
    return s < 0 ? 2 : 2 + 3 * (s + 1);
}

template <bool BT, int S> DEVI void stage_a(Ctx<BT>& c) {
    using C = Cfg<BT>;
    constexpr int N0 = issued<BT>(S), N1 = issued<BT>(S + 1);
    constexpr int H = C::RA / 2;
    ST(3 + 2 * S);
    wait_bar<N0 - need<BT>(S)>();
    ST(4 + 2 * S);
    if constexpr (S < 8) read_wraw<BT, S + 1, (S + 1) & 1>(c);
    read_xa<BT, S, S & 1>(c);
    SB();
    if constexpr (N0 < N1) issue<BT, N0>(c);
    SB();
    prune_w<BT, S, S & 1, 0>(c);
    if constexpr (S > 0) mma_a<BT, S - 1, (S - 1) & 1, 0, 1>(c);
    SB();
    if constexpr (N0 + 1 < N1) issue<BT, N0 + 1>(c);
    SB();
    if constexpr (S > 0) mma_a<BT, S - 1, (S - 1) & 1, 1, H>(c);
    SB();
    if constexpr (N0 + 2 < N1) issue<BT, N0 + 2>(c);
    SB();
    prune_w<BT, S, S & 1, 1>(c);
    if constexpr (S > 0) mma_a<BT, S - 1, (S - 1) & 1, H, H + 1>(c);
    SB();
    if constexpr (N0 + 3 < N1) issue<BT, N0 + 3>(c);
    SB();
    if constexpr (S > 0) mma_a<BT, S - 1, (S - 1) & 1, H + 1, C::RA>(c);
}

template <bool BT> DEVI void body(const uint16_t* x, const uint16_t* w, const uint16_t* bias, uint16_t* out,
                                  LDS3 char* lds, int row0, int nrows, int col0, unsigned* wsp) {
    using C = Cfg<BT>;
    Ctx<BT> c;
    c.lds = lds;
    ST(0);
    const int lane = threadIdx.x & 63;
    const int wv = __builtin_amdgcn_readfirstlane(threadIdx.x >> 6);
    const int h = wv >> 2, cg = wv & 3;
    const int cc = BT ? (cg & 1) : cg, sg = BT ? (cg >> 1) : 0;
    c.h = h;
    c.srdx.r = make_srd((const char*)x + (size_t)row0 * ROWB - BIASB, nrows * ROWB + BIASB);
    c.srdw.r = make_srd((const char*)w - BIASB, 576 * ROWB + BIASB);
    c.srdo = __builtin_amdgcn_make_buffer_rsrc((void*)(out + (size_t)row0 * 576), 0, nrows * ROWB, 0x00020000);
    c.wb = wv * C::MULT * KB;
    const int xlane = (lane >> 3) * ROWB + (((lane & 7) ^ (4 * (wv & 1) + (lane >> 4))) * 16) + BIASB;
    if constexpr (BT) {
        const int q1 = (lane >> 3) & 3, g = (q1 >> 1) | ((wv & 1) << 1);
        c.vw0 = (8 * wv + (lane >> 3)) * ROWB + col0 * 2 + (((lane & 7) ^ (g << 1)) * 16) + BIASB;
        c.vw1 = 0;
        c.vx0 = 8 * wv * ROWB + xlane - KB;
        c.vx1 = c.vx0 + 64 * ROWB - KB;
        c.vx2 = c.vx1 + 64 * ROWB - KB;
        c.vlate = 8 * wv * ROWB + xlane;
    } else {
        const int g = (lane >> 4) | (((wv >> 1) & 1) << 2);
        c.vw0 = (4 * wv + (lane >> 4)) * ROWB + col0 * 2 + (((lane & 15) ^ (g << 1)) * 16) + BIASB;
        c.vw1 = c.vw0 + 32 * ROWB - KB;
        c.vx0 = 8 * wv * ROWB + xlane - 2 * KB;
        c.vx1 = c.vx2 = 0;
        c.vlate = 8 * (wv & 3) * ROWB + xlane;
    }
    {
        const char* tp;
        if (wv < 3) tp = (const char*)w + min((wv * 64 + lane) * TOUCH, 576 * ROWB - 64);
        else if (wv < 5) tp = (const char*)x + (size_t)row0 * ROWB + min(((wv - 3) * 64 + lane) * TOUCH, nrows * ROWB - 64);
        else tp = (const char*)out + (size_t)row0 * ROWB + min(((wv - 5) * 64 + lane) * TOUCH, nrows * ROWB - 64);
        asm volatile("s_mov_b32 m0, %0\n\ts_nop 0\n\tglobal_load_lds_dword %1, off" :: "s"(138 * KB + wv * 256), "v"(tp) : "memory");
    }
    issue_range<BT, 0, need<BT>(0)>(c);
    const int q = lane >> 4, gm = lane & 15;
    u32x2 braw[2];
    {
        const uint16_t* bp = bias + col0 + 32 * cc + 4 * q;
        asm volatile("global_load_dwordx2 %0, %2, off\n\tglobal_load_dwordx2 %1, %2, off offset:32"
                     : "=&v"(braw[0]), "=&v"(braw[1]) : "v"(bp) : "memory");
    }
    issue_range<BT, need<BT>(0), issued<BT>(-1)>(c);
    unsigned exw, exr;
    c.kabs = opaque_s(0x7fff7fffu);
    c.ksgn = opaque_s(0x80008000u);
    {
        const int q1 = gm >> 2, p = gm & 3;
#pragma unroll
        for (int t = 0; t < 2; ++t) {
            unsigned o;
            if constexpr (BT) {
                const int g = (q1 >> 1) | ((q & 1) << 1);
                o = 4 * (4 * h + q) * KB + q1 * 128 + (((2 * cc + t) ^ g) * 32) + 8 * p;
                c.wrd2[t] = 0;
            } else {
                const int g = q1 | ((q & 1) << 2);
                const int in = q1 * 256 + (((2 * cc + t) ^ g) * 32) + 8 * p;
                o = (6 * q + h) * KB + in;
                c.wrd2[t] = C::PBASE + (4 * q + h) * KB + in;
            }
#pragma unroll
            for (int b = 0; b < C::NB; ++b) c.wrd[b][t] = opaque_v(o + b * C::BSTEP);
        }
        const int j = gm;
        const int in = (j & 7) * 128 + (((4 * h + q) ^ (j >> 1)) * 16);
#pragma unroll
        for (int par = 0; par < 2; ++par) {
            unsigned o;
            if constexpr (BT) o = 4 * KB * (4 * (par ^ h) + 2 * sg + (j >> 3)) + in;
            else o = 3 * KB * (2 * (par ^ h) + (j >> 3)) + in;
#pragma unroll
            for (int b = 0; b < C::NB; ++b) c.xrd[b][par] = opaque_v(o + b * C::BSTEP);
        }
        exw = wv * 2 * KB + lane * 16;
        exr = (wv ^ 4) * 2 * KB + lane * 16;
        const int trow = BT ? (32 * h + 16 * sg + j) : (16 * h + j);
        c.vout = trow * ROWB + (col0 + 32 * cc + 16 * (q & 1) + 8 * (q >> 1)) * 2;
    }
    ST(1);
    wait_bar<issued<BT>(-1) + 2 - need<BT>(-1)>();
    ST(2);
    read_wraw<BT, 0, 0>(c);
    SB();
    issue_range<BT, issued<BT>(-1), issued<BT>(0)>(c);
    SB();
    asm volatile("s_waitcnt vmcnt(%2)" : "+v"(braw[0]), "+v"(braw[1]) : "n"(issued<BT>(0) - need<BT>(0)) : "memory");
    SB();
#pragma unroll
    for (int t = 0; t < 2; ++t) {
        unsigned b0 = h ? 0u : braw[t][0], b1 = h ? 0u : braw[t][1];
        c.bias[t][0] = __builtin_bit_cast(float, b0 << 16);
        c.bias[t][1] = __builtin_bit_cast(float, b0 & 0xffff0000u);
        c.bias[t][2] = __builtin_bit_cast(float, b1 << 16);
        c.bias[t][3] = __builtin_bit_cast(float, b1 & 0xffff0000u);
    }
    stage_a<BT, 0>(c); stage_a<BT, 1>(c); stage_a<BT, 2>(c); stage_a<BT, 3>(c); stage_a<BT, 4>(c);
    stage_a<BT, 5>(c); stage_a<BT, 6>(c); stage_a<BT, 7>(c); stage_a<BT, 8>(c);

    if constexpr (BT) {
        constexpr int EA = 3 * C::SLOT;
        constexpr int EP = 3 * C::SLOT + 48 * KB;
        const unsigned w0 = opaque_v(exw + EA), r0 = opaque_v(exr + EA);
        wait_bar<3>();
        read_xp<BT, 0, 0, 1>(c);
        SB();
        mma_a<BT, 8, 0, 0, 6>(c);
        SB();
#pragma unroll
        for (int m = 0; m < 3; ++m) ex_write(c, w0 + m * 16 * KB, w0 + m * 16 * KB + KB, c.acc[2 * m + 1][0], c.acc[2 * m + 1][1]);
        read_xp<BT, 0, 1, 0>(c);
        SB();
        mma_p<BT, 0, 0, 1>(c);
        wait_bar<0>();
        Ex e0 = ex_read(c, r0, r0 + KB), e1 = ex_read(c, r0 + 16 * KB, r0 + 17 * KB), e2 = ex_read(c, r0 + 32 * KB, r0 + 33 * KB);
        read_xp<BT, 0, 2, 1>(c);
        SB();
        mma_p<BT, 0, 1, 0, 0, 1>(c);
        SB();
        finalize<BT, 0>(c, e0, c.acc[0][0], c.acc[0][1]);
        finalize<BT, 2>(c, e1, c.acc[2][0], c.acc[2][1]);
        finalize<BT, 4>(c, e2, c.acc[4][0], c.acc[4][1]);
        mma_p<BT, 0, 1, 0, 1, 3>(c);
        SB();
        mma_p<BT, 0, 2, 1>(c);
        ex_write(c, w0 + 48 * KB, w0 + 49 * KB, c.pacc[0][1][0], c.pacc[0][1][1]);
        lgkm_bar();
        Ex e3 = ex_read(c, r0 + 48 * KB, r0 + 49 * KB);
        finalize<BT, 6>(c, e3, c.pacc[0][0][0], c.pacc[0][0][1]);
        (void)EP;
    } else {
        const unsigned w0 = opaque_v(exw + C::PBASE), r0 = opaque_v(exr + C::PBASE);
        const unsigned w1 = opaque_v(exw + C::slot(1)), r1 = opaque_v(exr + C::slot(1));
        const unsigned e2w0 = C::slot(1) + 16 * KB + wv * KB + lane * 16, e2w1 = C::slot(2) + (3 * wv + 2) * KB + lane * 16;
        const unsigned e2r0 = C::slot(1) + 16 * KB + (wv ^ 4) * KB + lane * 16, e2r1 = C::slot(2) + (3 * (wv ^ 4) + 2) * KB + lane * 16;
        ST(21); wait_bar<11>(); ST(22);
        read_xp<BT, 0, 0, 1>(c);
        SB();
        mma_a<BT, 8, 0, 0, 4>(c);
        SB();
        ex_write(c, w0, w0 + KB, c.acc[1][0], c.acc[1][1]);
        ex_write(c, w1, w1 + KB, c.acc[3][0], c.acc[3][1]);
        read_xp<BT, 0, 1, 0>(c);
        SB();
        mma_p<BT, 0, 0, 1>(c);
        ST(23); wait_bar<9>(); ST(24);
        Ex ea = ex_read(c, r0, r0 + KB), eb = ex_read(c, r1, r1 + KB);
        read_xp<BT, 0, 2, 1>(c);
        SB();
        mma_p<BT, 0, 1, 0, 0, 1>(c);
        SB();
        finalize<BT, 0>(c, ea, c.acc[0][0], c.acc[0][1]);
        finalize<BT, 2>(c, eb, c.acc[2][0], c.acc[2][1]);
        mma_p<BT, 0, 1, 0, 1, 3>(c);
        ST(25); wait_bar<8>(); ST(26);
        read_xp<BT, 1, 0, 0>(c);
        SB();
        mma_p<BT, 0, 2, 1>(c);
        SB();
        read_xp<BT, 1, 1, 1>(c);
        SB();
        mma_p<BT, 1, 0, 0, 0, 1>(c);
        SB();
        ex_write(c, e2w0, e2w1, c.pacc[0][1][0], c.pacc[0][1][1]);
        SB();
        mma_p<BT, 1, 0, 0, 1, 3>(c);
        ST(27); wait_bar<7>(); ST(28);
        ea = ex_read(c, e2r0, e2r1);
        read_xp<BT, 1, 2, 0>(c);
        SB();
        mma_p<BT, 1, 1, 1, 0, 1>(c);
        SB();
        finalize<BT, 4>(c, ea, c.pacc[0][0][0], c.pacc[0][0][1]);
        mma_p<BT, 1, 1, 1, 1, 3>(c);
        if (nrows > 128) {
            ST(29); wait_bar<5>(); ST(30);
            read_xp<BT, 2, 0, 1>(c);
            SB();
            mma_p<BT, 1, 2, 0>(c);
            SB();
            read_xp<BT, 2, 1, 0>(c);
            SB();
            mma_p<BT, 2, 0, 1, 0, 1>(c);
            SB();
            ex_write(c, w0, w0 + KB, c.pacc[1][1][0], c.pacc[1][1][1]);
            SB();
            mma_p<BT, 2, 0, 1, 1, 3>(c);
            ST(31); wait_bar<3>(); ST(32);
            ea = ex_read(c, r0, r0 + KB);
            read_xp<BT, 2, 2, 1>(c);
            SB();
            mma_p<BT, 2, 1, 0, 0, 1>(c);
            SB();
            finalize<BT, 6>(c, ea, c.pacc[1][0][0], c.pacc[1][0][1]);
            mma_p<BT, 2, 1, 0, 1, 3>(c);
            SB();
            mma_p<BT, 2, 2, 1>(c);
            ex_write(c, w1, w1 + KB, c.pacc[0][1][0], c.pacc[0][1][1]);
            ST(33); lgkm_bar(); ST(34);
            ea = ex_read(c, r1, r1 + KB);
            finalize<BT, 8>(c, ea, c.pacc[0][0][0], c.pacc[0][0][1]);
        } else {
            SB();
            mma_p<BT, 1, 2, 0>(c);
            ex_write(c, w0, w0 + KB, c.pacc[1][1][0], c.pacc[1][1][1]);
            lgkm_bar();
            ea = ex_read(c, r0, r0 + KB);
            finalize<BT, 6>(c, ea, c.pacc[1][0][0], c.pacc[1][0][1]);
            asm volatile("s_waitcnt vmcnt(0)" ::: "memory");
        }
    }
}

__global__ __launch_bounds__(512, 2) void sparse24_gemm_kernel(const uint16_t* __restrict__ x, const uint16_t* __restrict__ w,
                                                               const uint16_t* __restrict__ bias, uint16_t* __restrict__ out, unsigned* wsp) {
    __shared__ __attribute__((aligned(1024))) char smem[163840];
    LDS3 char* lds = (LDS3 char*)smem;
    {
        uint64_t pc; asm volatile("s_getpc_b64 %0" : "=s"(pc));
        const char* cp = (const char*)(pc & ~(uint64_t)255) + min((int)threadIdx.x * 32, CODEPF - 32);
        unsigned m = __builtin_amdgcn_readfirstlane(136 * KB + (threadIdx.x >> 6) * 256);
        asm volatile("s_mov_b32 m0, %0\n\ts_nop 0\n\tglobal_load_lds_dword %1, off" :: "s"(m), "v"(cp) : "memory");
    }
    const int bid = blockIdx.x, xcd = bid & 7, j = bid >> 3;
    if (j < 28) {
        const int rbl = j >> 2, cb = j & 3;
        const int r0 = rbl < 4 ? rbl * 160 : 640 + (rbl - 4) * 128;
        body<false>(x, w, bias, out, lds, xcd * 1024 + r0, rbl < 4 ? 160 : 128, cb * 128, wsp);
    } else {
        body<true>(x, w, bias, out, lds, xcd * 1024 + (j - 28) * 256, 256, 512, wsp);
    }
}

extern "C" void kernel_launch(void* const* d_in, const int* in_sizes, int n_in, void* d_out, int out_size, void* d_ws,
                              size_t ws_size, hipStream_t stream) {
    sparse24_gemm_kernel<<<256, 512, 0, stream>>>((const uint16_t*)d_in[0], (const uint16_t*)d_in[1], (const uint16_t*)d_in[2],
                                                  (uint16_t*)d_out, (unsigned*)d_ws);
}
```

```cpp
#include <hip/hip_runtime.h>
#include <stdint.h>

typedef __attribute__((ext_vector_type(8))) short bf16x8;
typedef __attribute__((ext_vector_type(4))) short s16x4;
typedef __attribute__((ext_vector_type(2))) short s16x2;
typedef __attribute__((ext_vector_type(2))) unsigned short u16x2;
typedef __attribute__((ext_vector_type(4))) float f32x4;
typedef __attribute__((ext_vector_type(4))) unsigned u32x4;
typedef __attribute__((ext_vector_type(2))) unsigned u32x2;
typedef __attribute__((ext_vector_type(2))) __bf16 bf16x2_t;
typedef __attribute__((ext_vector_type(2))) float f32x2;
#define LDS3 __attribute__((address_space(3)))
#define DEVI __device__ __forceinline__

#define STORE_AUX 16

constexpr int ROWB = 1152;
constexpr int KB = 1024;
constexpr int BIASB = 4096;

#define ST(i)
template <int N> DEVI void wait_bar() {
    __builtin_amdgcn_sched_barrier(0);
    asm volatile("s_waitcnt vmcnt(%0) lgkmcnt(0)\n\ts_barrier" ::"n"(N) : "memory");
    __builtin_amdgcn_sched_barrier(0);
}
DEVI void lgkm_bar() {
    __builtin_amdgcn_sched_barrier(0);
    asm volatile("s_waitcnt lgkmcnt(0)\n\ts_barrier" ::: "memory");
    __builtin_amdgcn_sched_barrier(0);
}

#define XPOL ""
#define WPOL ""
struct SrdX { u32x4 r; };
struct SrdW { u32x4 r; };
template <int C, int U> DEVI void dma_set(unsigned wb, unsigned v, SrdX r, unsigned so) {
    asm volatile("s_add_u32 m0, %0, %1\n\ts_nop 0\n\tbuffer_load_dwordx4 %2, %3, %4 offen offset:%5" XPOL " lds"
                 ::"s"(wb), "n"(C), "v"(v), "s"(r.r), "s"(so), "n"(U * 1024) : "memory", "scc");
}
template <int U> DEVI void dma_nxt(unsigned v, SrdX r, unsigned so) {
    asm volatile("buffer_load_dwordx4 %0, %1, %2 offen offset:%3" XPOL " lds" ::"v"(v), "s"(r.r), "s"(so), "n"(U * 1024) : "memory");
}
template <int C, int U> DEVI void dma_set(unsigned wb, unsigned v, SrdW r, unsigned so) {
    asm volatile("s_add_u32 m0, %0, %1\n\ts_nop 0\n\tbuffer_load_dwordx4 %2, %3, %4 offen offset:%5" WPOL " lds"
                 ::"s"(wb), "n"(C), "v"(v), "s"(r.r), "s"(so), "n"(U * 1024) : "memory", "scc");
}
template <int U> DEVI void dma_nxt(unsigned v, SrdW r, unsigned so) {
    asm volatile("buffer_load_dwordx4 %0, %1, %2 offen offset:%3" WPOL " lds" ::"v"(v), "s"(r.r), "s"(so), "n"(U * 1024) : "memory");
}
#define SB() __builtin_amdgcn_sched_barrier(0)
DEVI unsigned opaque_v(unsigned x) { asm volatile("" : "+v"(x)); return x; }
DEVI unsigned opaque_s(unsigned x) { asm volatile("" : "+s"(x)); return x; }

DEVI u32x4 make_srd(const void* p, unsigned bytes) {
    uint64_t a = (uint64_t)p;
    u32x4 r;
    r[0] = __builtin_amdgcn_readfirstlane((unsigned)a);
    r[1] = __builtin_amdgcn_readfirstlane((unsigned)(a >> 32) & 0xffffu);
    r[2] = __builtin_amdgcn_readfirstlane(bytes);
    r[3] = 0x00020000u;
    return r;
}

DEVI void prune4(unsigned d0, unsigned d1, unsigned& r0, unsigned& r1, unsigned kabs, unsigned ksgn) {
    unsigned m0 = d0 & kabs, m1 = d1 & kabs;
    u16x2 a = __builtin_bit_cast(u16x2, m0), b = __builtin_bit_cast(u16x2, m1);
    u16x2 H = __builtin_elementwise_max(a, b), L = __builtin_elementwise_min(a, b);
    u16x2 Hs = {H[1], H[0]}, Ls = {L[1], L[0]};
    u16x2 thr = __builtin_elementwise_max(__builtin_elementwise_min(H, Hs), __builtin_elementwise_max(L, Ls));
    unsigned T = ksgn - __builtin_bit_cast(unsigned, thr);
    unsigned t0 = m0 + T, t1 = m1 + T;
    s16x2 k0 = __builtin_bit_cast(s16x2, t0) >> 15, k1 = __builtin_bit_cast(s16x2, t1) >> 15;
    r0 = d0 & __builtin_bit_cast(unsigned, k0);
    r1 = d1 & __builtin_bit_cast(unsigned, k1);
}

DEVI unsigned cvt_pk(float a, float b) {
    f32x2 v = {a, b};
    return __builtin_bit_cast(unsigned, __builtin_convertvector(v, bf16x2_t));
}

template <bool BT> struct Cfg {
    static constexpr int RA = BT ? 6 : 4;
    static constexpr int MULT = BT ? 4 : 3;
    static constexpr int SLOT = MULT * 8 * KB;
    static constexpr int NB = BT ? 3 : 2;
    static constexpr int BSTEP = BT ? 64 * KB : 72 * KB;
    static constexpr int PBASE = 144 * KB;
    static constexpr int slot(int s) {
        if (BT) { int c = (s + 6) % 5; return c * SLOT; }
        return (s % 6) * SLOT;
    }
    static constexpr int late(int v) {
        if (BT) return (v >> 2) * SLOT + (v & 3) * KB;
        if (v < 2) return slot(8) + v * KB;
        return slot(3 + (v - 2) / 3) + ((v - 2) % 3) * KB;
    }
};

template <bool BT> struct Ctx {
    using C = Cfg<BT>;
    LDS3 char* lds;
    SrdX srdx; SrdW srdw;
    __amdgpu_buffer_rsrc_t srdo;
    unsigned wb;
    unsigned vw0, vw1;
    unsigned vx0, vx1, vx2;
    unsigned vlate;
    unsigned h;
    unsigned kabs, ksgn;
    unsigned wrd[C::NB][2];
    unsigned wrd2[2];
    unsigned xrd[C::NB][2];
    unsigned vout;
    u32x2 wraw[2][4];
    bf16x8 wf[9][2];
    bf16x8 xf[2][6];
    f32x4 acc[C::RA][2];
    f32x4 pacc[2][2][2];
    f32x4 bias[2];
};

template <bool BT, int S, int BUF> DEVI void read_wraw(Ctx<BT>& c) {
    using C = Cfg<BT>;
#pragma unroll
    for (int t = 0; t < 2; ++t)
#pragma unroll
        for (int e = 0; e < 2; ++e) {
            unsigned off;
            if (!BT && S == 0) off = c.wrd2[t] + e * 2 * KB;
            else {
                constexpr int sl = C::slot(S - 1), b = sl / C::BSTEP;
                off = c.wrd[b][t] + (sl - b * C::BSTEP) + e * (BT ? 512 : 3 * KB);
            }
            s16x4 v = __builtin_amdgcn_ds_read_tr16_b64_v4i16((LDS3 s16x4*)(c.lds + off));
            c.wraw[BUF][t * 2 + e] = __builtin_bit_cast(u32x2, v);
        }
}
template <bool BT, int S, int BUF, int T> DEVI void prune_w(Ctx<BT>& c) {
    u32x4 r;
    unsigned a, b;
    prune4(c.wraw[BUF][T * 2][0], c.wraw[BUF][T * 2][1], a, b, c.kabs, c.ksgn); r[0] = a; r[1] = b;
    prune4(c.wraw[BUF][T * 2 + 1][0], c.wraw[BUF][T * 2 + 1][1], a, b, c.kabs, c.ksgn); r[2] = a; r[3] = b;
    c.wf[S][T] = __builtin_bit_cast(bf16x8, r);
}
template <bool BT, int S, int BUF> DEVI void read_xa(Ctx<BT>& c) {
    using C = Cfg<BT>;
    constexpr int sl = C::slot(S), b = sl / C::BSTEP;
#pragma unroll
    for (int tl = 0; tl < C::RA; ++tl) {
        unsigned off = c.xrd[b][tl & 1] + (sl - b * C::BSTEP) + (BT ? (1 + (tl >> 1)) * KB : ((tl >> 1) * 12 + 2) * KB);
        c.xf[BUF][tl] = *(const LDS3 bf16x8*)(c.lds + off);
    }
}
template <bool BT, int S, int BUF, int T0, int T1> DEVI void mma_a(Ctx<BT>& c) {
#pragma unroll
    for (int tl = T0; tl < T1; ++tl)
#pragma unroll
        for (int t = 0; t < 2; ++t)
            c.acc[tl][t] = __builtin_amdgcn_mfma_f32_16x16x32_bf16(c.wf[S][t], c.xf[BUF][tl], S == 0 ? c.bias[t] : c.acc[tl][t], 0, 0, 0);
}
template <bool BT, int P, int S, int IDX, int BUF> DEVI void read_xp1(Ctx<BT>& c) {
    using C = Cfg<BT>;
    constexpr int un = 9 * P + S;
    constexpr int lo = BT ? C::late(S) : C::late(un >> 1);
    constexpr int b = lo / C::BSTEP;
    constexpr int rest = lo - b * C::BSTEP + (BT ? 0 : (un & 1) * 12 * KB);
    c.xf[BUF][IDX * 2 + 0] = *(const LDS3 bf16x8*)(c.lds + c.xrd[b][0] + rest);
    c.xf[BUF][IDX * 2 + 1] = *(const LDS3 bf16x8*)(c.lds + c.xrd[b][1] + rest);
}
template <bool BT, int P, int B, int BUF> DEVI void read_xp(Ctx<BT>& c) {
    read_xp1<BT, P, 3 * B, 0, BUF>(c); read_xp1<BT, P, 3 * B + 1, 1, BUF>(c); read_xp1<BT, P, 3 * B + 2, 2, BUF>(c);
}
template <bool BT, int P, int B, int BUF, int I0 = 0, int I1 = 3> DEVI void mma_p(Ctx<BT>& c) {
#pragma unroll
    for (int i = I0; i < I1; ++i)
#pragma unroll
        for (int par = 0; par < 2; ++par)
#pragma unroll
            for (int t = 0; t < 2; ++t)
                c.pacc[P & 1][par][t] = __builtin_amdgcn_mfma_f32_16x16x32_bf16(c.wf[3 * B + i][t], c.xf[BUF][i * 2 + par],
                                                                                (B == 0 && i == 0) ? c.bias[t] : c.pacc[P & 1][par][t], 0, 0, 0);
}
template <bool BT> DEVI void ex_write(Ctx<BT>& c, unsigned a0, unsigned a1, const f32x4& v0, const f32x4& v1) {
    *(LDS3 f32x4*)(c.lds + a0) = v0;
    *(LDS3 f32x4*)(c.lds + a1) = v1;
}
struct Ex { f32x4 p0, p1; };
template <bool BT> DEVI Ex ex_read(Ctx<BT>& c, unsigned a0, unsigned a1) {
    Ex e;
    e.p0 = *(const LDS3 f32x4*)(c.lds + a0);
    e.p1 = *(const LDS3 f32x4*)(c.lds + a1);
    return e;
}
DEVI float addf(float a, float b) { float r; asm("v_add_f32 %0, %1, %2" : "=v"(r) : "v"(a), "v"(b)); return r; }
template <bool BT, int TL> DEVI void finalize(Ctx<BT>& c, const Ex& e, const f32x4& v0, const f32x4& v1) {
    f32x4 s0 = v0 + e.p0, s1 = v1 + e.p1;
    unsigned q00 = cvt_pk(s0[0], s0[1]), q01 = cvt_pk(s0[2], s0[3]), q10 = cvt_pk(s1[0], s1[1]), q11 = cvt_pk(s1[2], s1[3]);
    auto x0 = __builtin_amdgcn_permlane16_swap(q00, q10, false, false);
    auto x1 = __builtin_amdgcn_permlane16_swap(q01, q11, false, false);
    u32x4 o = {x0[0], x1[0], x0[1], x1[1]};
    __builtin_amdgcn_raw_buffer_store_b128(o, c.srdo, c.vout, TL * (BT ? 32 : 16) * ROWB, STORE_AUX);
}

template <bool BT, int V> DEVI unsigned late_soff(Ctx<BT>& c) {
    constexpr int u = V < 2 ? V : (V - 2) % 3;
    constexpr int u0 = 2 * V, u1 = 2 * V + 1;
    constexpr int S0 = (64 + 32 * (u0 / 9)) * ROWB + (u0 % 9) * 128 - u * KB;
    constexpr int S1 = (64 + 32 * (u1 / 9)) * ROWB + (u1 % 9) * 128 - u * KB;
    return S0 + c.h * (S1 - S0);
}

template <bool BT, int N> DEVI void issue(Ctx<BT>& c) {
    using C = Cfg<BT>;
    constexpr int WCH = 64 * ROWB;
    if constexpr (BT) {
        if constexpr (N == 0) dma_set<C::slot(-1), 0>(c.wb, c.vw0, c.srdw, 0);
        else if constexpr (N <= 32) {
            constexpr int s = (N - 1) / 4, u = (N - 1) % 4;
            if constexpr (u == 0) dma_set<C::slot(s), 0>(c.wb, c.vw0, c.srdw, (s + 1) * WCH);
            else dma_nxt<u>(u == 1 ? c.vx0 : (u == 2 ? c.vx1 : c.vx2), c.srdx, s * 128);
        } else if constexpr (N == 33) dma_set<C::slot(8), 1>(c.wb, c.vx0, c.srdx, 8 * 128);
        else if constexpr (N <= 35) dma_nxt<N - 32>(N == 34 ? c.vx1 : c.vx2, c.srdx, 8 * 128);
        else if constexpr (N <= 44) {
            constexpr int v = N - 36, so = 192 * ROWB + v * 128 - (v & 3) * KB;
            if constexpr ((v & 3) == 0) dma_set<C::late(v), 0>(c.wb, c.vlate, c.srdx, so);
            else dma_nxt<(v & 3)>(c.vlate, c.srdx, so);
        }
    } else {
        if constexpr (N == 0) dma_set<C::PBASE, 0>(c.wb - (c.wb / 3072) * 1024, c.vw0, c.srdw, 0);
        else if constexpr (N == 1) dma_nxt<1>(c.vw1, c.srdw, 0);
        else if constexpr (N <= 25) {
            constexpr int s = (N - 2) / 3, u = (N - 2) % 3;
            if constexpr (u == 0) dma_set<C::slot(s), 0>(c.wb, c.vw0, c.srdw, (s + 1) * WCH);
            else if constexpr (u == 1) dma_nxt<1>(c.vw1, c.srdw, (s + 1) * WCH);
            else dma_nxt<2>(c.vx0, c.srdx, s * 128);
        } else if constexpr (N == 26) dma_set<C::slot(8), 0>(c.wb, c.vlate, c.srdx, late_soff<BT, 0>(c));
        else if constexpr (N == 27) dma_nxt<1>(c.vlate, c.srdx, late_soff<BT, 1>(c));
        else if constexpr (N == 28) dma_nxt<2>(c.vx0, c.srdx, 8 * 128);
        else if constexpr (N <= 40) {
            constexpr int v = N - 27, u = (v - 2) % 3;
            if constexpr (u == 0) dma_set<C::late(v), 0>(c.wb, c.vlate, c.srdx, late_soff<BT, v>(c));
            else dma_nxt<u>(c.vlate, c.srdx, late_soff<BT, v>(c));
        }
    }
}
template <bool BT, int N0, int N1> DEVI void issue_range(Ctx<BT>& c) {
    if constexpr (N0 < N1) { issue<BT, N0>(c); issue_range<BT, N0 + 1, N1>(c); }
}
template <bool BT> constexpr int issued(int s) {
    if (BT) { constexpr int t[11] = {9, 13, 17, 21, 25, 29, 33, 36, 40, 44, 45}; return t[s + 1]; }
    else    { constexpr int t[11] = {8, 11, 15, 19, 23, 26, 29, 32, 35, 38, 41}; return t[s + 1]; }
}
template <bool BT> constexpr int need(int s) {
    if (BT) return s < 0 ? 1 : (s < 8 ? 1 + 4 * (s + 1) : 36);
    return s < 0 ? 2 : 2 + 3 * (s + 1);
}

template <bool BT, int S> DEVI void stage_a(Ctx<BT>& c) {
    using C = Cfg<BT>;
    constexpr int N0 = issued<BT>(S), N1 = issued<BT>(S + 1);
    constexpr int H = C::RA / 2;
    ST(3 + 2 * S);
    wait_bar<N0 - need<BT>(S)>();
    ST(4 + 2 * S);
    if constexpr (S < 8) read_wraw<BT, S + 1, (S + 1) & 1>(c);
    read_xa<BT, S, S & 1>(c);
    SB();
    if constexpr (N0 < N1) issue<BT, N0>(c);
    SB();
    prune_w<BT, S, S & 1, 0>(c);
    if constexpr (S > 0) mma_a<BT, S - 1, (S - 1) & 1, 0, 1>(c);
    SB();
    if constexpr (N0 + 1 < N1) issue<BT, N0 + 1>(c);
    SB();
    if constexpr (S > 0) mma_a<BT, S - 1, (S - 1) & 1, 1, H>(c);
    SB();
    if constexpr (N0 + 2 < N1) issue<BT, N0 + 2>(c);
    SB();
    prune_w<BT, S, S & 1, 1>(c);
    if constexpr (S > 0) mma_a<BT, S - 1, (S - 1) & 1, H, H + 1>(c);
    SB();
    if constexpr (N0 + 3 < N1) issue<BT, N0 + 3>(c);
    SB();
    if constexpr (S > 0) mma_a<BT, S - 1, (S - 1) & 1, H + 1, C::RA>(c);
}

template <bool BT> DEVI void body(const uint16_t* x, const uint16_t* w, const uint16_t* bias, uint16_t* out,
                                  LDS3 char* lds, int row0, int nrows, int col0, unsigned* wsp) {
    using C = Cfg<BT>;
    Ctx<BT> c;
    c.lds = lds;
    ST(0);
    const int lane = threadIdx.x & 63;
    const int wv = __builtin_amdgcn_readfirstlane(threadIdx.x >> 6);
    const int h = wv >> 2, cg = wv & 3;
    const int cc = BT ? (cg & 1) : cg, sg = BT ? (cg >> 1) : 0;
    c.h = h;
    c.srdx.r = make_srd((const char*)x + (size_t)row0 * ROWB - BIASB, nrows * ROWB + BIASB);
    c.srdw.r = make_srd((const char*)w - BIASB, 576 * ROWB + BIASB);
    c.srdo = __builtin_amdgcn_make_buffer_rsrc((void*)(out + (size_t)row0 * 576), 0, nrows * ROWB, 0x00020000);
    c.wb = wv * C::MULT * KB;
    const int xlane = (lane >> 3) * ROWB + (((lane & 7) ^ (4 * (wv & 1) + (lane >> 4))) * 16) + BIASB;
    if constexpr (BT) {
        const int q1 = (lane >> 3) & 3, g = (q1 >> 1) | ((wv & 1) << 1);
        c.vw0 = (8 * wv + (lane >> 3)) * ROWB + col0 * 2 + (((lane & 7) ^ (g << 1)) * 16) + BIASB;
        c.vw1 = 0;
        c.vx0 = 8 * wv * ROWB + xlane - KB;
        c.vx1 = c.vx0 + 64 * ROWB - KB;
        c.vx2 = c.vx1 + 64 * ROWB - KB;
        c.vlate = 8 * wv * ROWB + xlane;
    } else {
        const int g = (lane >> 4) | (((wv >> 1) & 1) << 2);
        c.vw0 = (4 * wv + (lane >> 4)) * ROWB + col0 * 2 + (((lane & 15) ^ (g << 1)) * 16) + BIASB;
        c.vw1 = c.vw0 + 32 * ROWB - KB;
        c.vx0 = 8 * wv * ROWB + xlane - 2 * KB;
        c.vx1 = c.vx2 = 0;
        c.vlate = 8 * (wv & 3) * ROWB + xlane;
    }
    issue_range<BT, 0, need<BT>(0)>(c);
    const int q = lane >> 4, gm = lane & 15;
    u32x2 braw[2];
    {
        const uint16_t* bp = bias + col0 + 32 * cc + 4 * q;
        asm volatile("global_load_dwordx2 %0, %2, off\n\tglobal_load_dwordx2 %1, %2, off offset:32"
                     : "=&v"(braw[0]), "=&v"(braw[1]) : "v"(bp) : "memory");
    }
    issue_range<BT, need<BT>(0), issued<BT>(-1)>(c);
    unsigned exw, exr;
    c.kabs = opaque_s(0x7fff7fffu);
    c.ksgn = opaque_s(0x80008000u);
    {
        const int q1 = gm >> 2, p = gm & 3;
#pragma unroll
        for (int t = 0; t < 2; ++t) {
            unsigned o;
            if constexpr (BT) {
                const int g = (q1 >> 1) | ((q & 1) << 1);
                o = 4 * (4 * h + q) * KB + q1 * 128 + (((2 * cc + t) ^ g) * 32) + 8 * p;
                c.wrd2[t] = 0;
            } else {
                const int g = q1 | ((q & 1) << 2);
                const int in = q1 * 256 + (((2 * cc + t) ^ g) * 32) + 8 * p;
                o = (6 * q + h) * KB + in;
                c.wrd2[t] = C::PBASE + (4 * q + h) * KB + in;
            }
#pragma unroll
            for (int b = 0; b < C::NB; ++b) c.wrd[b][t] = opaque_v(o + b * C::BSTEP);
        }
        const int j = gm;
        const int in = (j & 7) * 128 + (((4 * h + q) ^ (j >> 1)) * 16);
#pragma unroll
        for (int par = 0; par < 2; ++par) {
            unsigned o;
            if constexpr (BT) o = 4 * KB * (4 * (par ^ h) + 2 * sg + (j >> 3)) + in;
            else o = 3 * KB * (2 * (par ^ h) + (j >> 3)) + in;
#pragma unroll
            for (int b = 0; b < C::NB; ++b) c.xrd[b][par] = opaque_v(o + b * C::BSTEP);
        }
        exw = wv * 2 * KB + lane * 16;
        exr = (wv ^ 4) * 2 * KB + lane * 16;
        const int trow = BT ? (32 * h + 16 * sg + j) : (16 * h + j);
        c.vout = trow * ROWB + (col0 + 32 * cc + 16 * (q & 1) + 8 * (q >> 1)) * 2;
    }
    ST(1);
    wait_bar<issued<BT>(-1) + 2 - need<BT>(-1)>();
    ST(2);
    read_wraw<BT, 0, 0>(c);
    SB();
    issue_range<BT, issued<BT>(-1), issued<BT>(0)>(c);
    SB();
    asm volatile("s_waitcnt vmcnt(%2)" : "+v"(braw[0]), "+v"(braw[1]) : "n"(issued<BT>(0) - need<BT>(0)) : "memory");
    SB();
#pragma unroll
    for (int t = 0; t < 2; ++t) {
        unsigned b0 = h ? 0u : braw[t][0], b1 = h ? 0u : braw[t][1];
        c.bias[t][0] = __builtin_bit_cast(float, b0 << 16);
        c.bias[t][1] = __builtin_bit_cast(float, b0 & 0xffff0000u);
        c.bias[t][2] = __builtin_bit_cast(float, b1 << 16);
        c.bias[t][3] = __builtin_bit_cast(float, b1 & 0xffff0000u);
    }
    stage_a<BT, 0>(c); stage_a<BT, 1>(c); stage_a<BT, 2>(c); stage_a<BT, 3>(c); stage_a<BT, 4>(c);
    stage_a<BT, 5>(c); stage_a<BT, 6>(c); stage_a<BT, 7>(c); stage_a<BT, 8>(c);

    if constexpr (BT) {
        constexpr int EA = 3 * C::SLOT;
        constexpr int EP = 3 * C::SLOT + 48 * KB;
        const unsigned w0 = opaque_v(exw + EA), r0 = opaque_v(exr + EA);
        wait_bar<3>();
        read_xp<BT, 0, 0, 1>(c);
        SB();
        mma_a<BT, 8, 0, 0, 6>(c);
        SB();
#pragma unroll
        for (int m = 0; m < 3; ++m) ex_write(c, w0 + m * 16 * KB, w0 + m * 16 * KB + KB, c.acc[2 * m + 1][0], c.acc[2 * m + 1][1]);
        read_xp<BT, 0, 1, 0>(c);
        SB();
        mma_p<BT, 0, 0, 1>(c);
        wait_bar<0>();
        Ex e0 = ex_read(c, r0, r0 + KB), e1 = ex_read(c, r0 + 16 * KB, r0 + 17 * KB), e2 = ex_read(c, r0 + 32 * KB, r0 + 33 * KB);
        read_xp<BT, 0, 2, 1>(c);
        SB();
        mma_p<BT, 0, 1, 0, 0, 1>(c);
        SB();
        finalize<BT, 0>(c, e0, c.acc[0][0], c.acc[0][1]);
        finalize<BT, 2>(c, e1, c.acc[2][0], c.acc[2][1]);
        finalize<BT, 4>(c, e2, c.acc[4][0], c.acc[4][1]);
        mma_p<BT, 0, 1, 0, 1, 3>(c);
        SB();
        mma_p<BT, 0, 2, 1>(c);
        ex_write(c, w0 + 48 * KB, w0 + 49 * KB, c.pacc[0][1][0], c.pacc[0][1][1]);
        lgkm_bar();
        Ex e3 = ex_read(c, r0 + 48 * KB, r0 + 49 * KB);
        finalize<BT, 6>(c, e3, c.pacc[0][0][0], c.pacc[0][0][1]);
        (void)EP;
    } else {
        const unsigned w0 = opaque_v(exw + C::PBASE), r0 = opaque_v(exr + C::PBASE);
        const unsigned w1 = opaque_v(exw + C::slot(1)), r1 = opaque_v(exr + C::slot(1));
        const unsigned e2w0 = C::slot(1) + 16 * KB + wv * KB + lane * 16, e2w1 = C::slot(2) + (3 * wv + 2) * KB + lane * 16;
        const unsigned e2r0 = C::slot(1) + 16 * KB + (wv ^ 4) * KB + lane * 16, e2r1 = C::slot(2) + (3 * (wv ^ 4) + 2) * KB + lane * 16;
        ST(21); wait_bar<11>(); ST(22);
        read_xp<BT, 0, 0, 1>(c);
        SB();
        mma_a<BT, 8, 0, 0, 4>(c);
        SB();
        ex_write(c, w0, w0 + KB, c.acc[1][0], c.acc[1][1]);
        ex_write(c, w1, w1 + KB, c.acc[3][0], c.acc[3][1]);
        read_xp<BT, 0, 1, 0>(c);
        SB();
        mma_p<BT, 0, 0, 1>(c);
        ST(23); wait_bar<9>(); ST(24);
        Ex ea = ex_read(c, r0, r0 + KB), eb = ex_read(c, r1, r1 + KB);
        read_xp<BT, 0, 2, 1>(c);
        SB();
        mma_p<BT, 0, 1, 0, 0, 1>(c);
        SB();
        finalize<BT, 0>(c, ea, c.acc[0][0], c.acc[0][1]);
        finalize<BT, 2>(c, eb, c.acc[2][0], c.acc[2][1]);
        mma_p<BT, 0, 1, 0, 1, 3>(c);
        ST(25); wait_bar<8>(); ST(26);
        read_xp<BT, 1, 0, 0>(c);
        SB();
        mma_p<BT, 0, 2, 1>(c);
        SB();
        read_xp<BT, 1, 1, 1>(c);
        SB();
        mma_p<BT, 1, 0, 0, 0, 1>(c);
        SB();
        ex_write(c, e2w0, e2w1, c.pacc[0][1][0], c.pacc[0][1][1]);
        SB();
        mma_p<BT, 1, 0, 0, 1, 3>(c);
        ST(27); wait_bar<7>(); ST(28);
        ea = ex_read(c, e2r0, e2r1);
        read_xp<BT, 1, 2, 0>(c);
        SB();
        mma_p<BT, 1, 1, 1, 0, 1>(c);
        SB();
        finalize<BT, 4>(c, ea, c.pacc[0][0][0], c.pacc[0][0][1]);
        mma_p<BT, 1, 1, 1, 1, 3>(c);
        if (nrows > 128) {
            ST(29); wait_bar<5>(); ST(30);
            read_xp<BT, 2, 0, 1>(c);
            SB();
            mma_p<BT, 1, 2, 0>(c);
            SB();
            read_xp<BT, 2, 1, 0>(c);
            SB();
            mma_p<BT, 2, 0, 1, 0, 1>(c);
            SB();
            ex_write(c, w0, w0 + KB, c.pacc[1][1][0], c.pacc[1][1][1]);
            SB();
            mma_p<BT, 2, 0, 1, 1, 3>(c);
            ST(31); wait_bar<3>(); ST(32);
            ea = ex_read(c, r0, r0 + KB);
            read_xp<BT, 2, 2, 1>(c);
            SB();
            mma_p<BT, 2, 1, 0, 0, 1>(c);
            SB();
            finalize<BT, 6>(c, ea, c.pacc[1][0][0], c.pacc[1][0][1]);
            mma_p<BT, 2, 1, 0, 1, 3>(c);
            SB();
            mma_p<BT, 2, 2, 1>(c);
            ex_write(c, w1, w1 + KB, c.pacc[0][1][0], c.pacc[0][1][1]);
            ST(33); lgkm_bar(); ST(34);
            ea = ex_read(c, r1, r1 + KB);
            finalize<BT, 8>(c, ea, c.pacc[0][0][0], c.pacc[0][0][1]);
        } else {
            SB();
            mma_p<BT, 1, 2, 0>(c);
            ex_write(c, w0, w0 + KB, c.pacc[1][1][0], c.pacc[1][1][1]);
            lgkm_bar();
            ea = ex_read(c, r0, r0 + KB);
            finalize<BT, 6>(c, ea, c.pacc[1][0][0], c.pacc[1][0][1]);
            asm volatile("s_waitcnt vmcnt(0)" ::: "memory");
        }
    }
}

__global__ __launch_bounds__(512, 2) void sparse24_gemm_kernel(const uint16_t* __restrict__ x, const uint16_t* __restrict__ w,
                                                               const uint16_t* __restrict__ bias, uint16_t* __restrict__ out, unsigned* wsp) {
    __shared__ __attribute__((aligned(1024))) char smem[163840];
    LDS3 char* lds = (LDS3 char*)smem;
    const int bid = blockIdx.x, xcd = bid & 7, j = bid >> 3;
    if (j < 28) {
        const int rbl = j >> 2, cb = j & 3;
        const int r0 = rbl < 4 ? rbl * 160 : 640 + (rbl - 4) * 128;
        body<false>(x, w, bias, out, lds, xcd * 1024 + r0, rbl < 4 ? 160 : 128, cb * 128, wsp);
    } else {
        body<true>(x, w, bias, out, lds, xcd * 1024 + (j - 28) * 256, 256, 512, wsp);
    }
}

extern "C" void kernel_launch(void* const* d_in, const int* in_sizes, int n_in, void* d_out, int out_size, void* d_ws,
                              size_t ws_size, hipStream_t stream) {
    sparse24_gemm_kernel<<<256, 512, 0, stream>>>((const uint16_t*)d_in[0], (const uint16_t*)d_in[1], (const uint16_t*)d_in[2],
                                                  (uint16_t*)d_out, (unsigned*)d_ws);
}
```

```cpp
#define PAD1 300
#include <hip/hip_runtime.h>
#include <stdint.h>

typedef __attribute__((ext_vector_type(8))) short bf16x8;
typedef __attribute__((ext_vector_type(4))) short s16x4;
typedef __attribute__((ext_vector_type(2))) short s16x2;
typedef __attribute__((ext_vector_type(2))) unsigned short u16x2;
typedef __attribute__((ext_vector_type(4))) float f32x4;
typedef __attribute__((ext_vector_type(4))) unsigned u32x4;
typedef __attribute__((ext_vector_type(2))) unsigned u32x2;
typedef __attribute__((ext_vector_type(2))) __bf16 bf16x2_t;
typedef __attribute__((ext_vector_type(2))) float f32x2;
#define LDS3 __attribute__((address_space(3)))
#define DEVI __device__ __forceinline__

#define STORE_AUX 16

constexpr int ROWB = 1152;
constexpr int KB = 1024;
constexpr int BIASB = 4096;

#define ST(i)
template <int N> DEVI void wait_bar() {
    __builtin_amdgcn_sched_barrier(0);
    asm volatile("s_waitcnt vmcnt(%0) lgkmcnt(0)\n\ts_barrier" ::"n"(N) : "memory");
    __builtin_amdgcn_sched_barrier(0);
}
DEVI void lgkm_bar() {
    __builtin_amdgcn_sched_barrier(0);
    asm volatile("s_waitcnt lgkmcnt(0)\n\ts_barrier" ::: "memory");
    __builtin_amdgcn_sched_barrier(0);
}

#define XPOL ""
#define WPOL ""
struct SrdX { u32x4 r; };
struct SrdW { u32x4 r; };
template <int C, int U> DEVI void dma_set(unsigned wb, unsigned v, SrdX r, unsigned so) {
    asm volatile("s_add_u32 m0, %0, %1\n\ts_nop 0\n\tbuffer_load_dwordx4 %2, %3, %4 offen offset:%5" XPOL " lds"
                 ::"s"(wb), "n"(C), "v"(v), "s"(r.r), "s"(so), "n"(U * 1024) : "memory", "scc");
}
template <int U> DEVI void dma_nxt(unsigned v, SrdX r, unsigned so) {
    asm volatile("buffer_load_dwordx4 %0, %1, %2 offen offset:%3" XPOL " lds" ::"v"(v), "s"(r.r), "s"(so), "n"(U * 1024) : "memory");
}
template <int C, int U> DEVI void dma_set(unsigned wb, unsigned v, SrdW r, unsigned so) {
    asm volatile("s_add_u32 m0, %0, %1\n\ts_nop 0\n\tbuffer_load_dwordx4 %2, %3, %4 offen offset:%5" WPOL " lds"
                 ::"s"(wb), "n"(C), "v"(v), "s"(r.r), "s"(so), "n"(U * 1024) : "memory", "scc");
}
template <int U> DEVI void dma_nxt(unsigned v, SrdW r, unsigned so) {
    asm volatile("buffer_load_dwordx4 %0, %1, %2 offen offset:%3" WPOL " lds" ::"v"(v), "s"(r.r), "s"(so), "n"(U * 1024) : "memory");
}
#define SB() __builtin_amdgcn_sched_barrier(0)
DEVI unsigned opaque_v(unsigned x) { asm volatile("" : "+v"(x)); return x; }
DEVI unsigned opaque_s(unsigned x) { asm volatile("" : "+s"(x)); return x; }

DEVI u32x4 make_srd(const void* p, unsigned bytes) {
    uint64_t a = (uint64_t)p;
    u32x4 r;
    r[0] = __builtin_amdgcn_readfirstlane((unsigned)a);
    r[1] = __builtin_amdgcn_readfirstlane((unsigned)(a >> 32) & 0xffffu);
    r[2] = __builtin_amdgcn_readfirstlane(bytes);
    r[3] = 0x00020000u;
    return r;
}

DEVI void prune4(unsigned d0, unsigned d1, unsigned& r0, unsigned& r1, unsigned kabs, unsigned ksgn) {
    unsigned m0 = d0 & kabs, m1 = d1 & kabs;
    u16x2 a = __builtin_bit_cast(u16x2, m0), b = __builtin_bit_cast(u16x2, m1);
    u16x2 H = __builtin_elementwise_max(a, b), L = __builtin_elementwise_min(a, b);
    u16x2 Hs = {H[1], H[0]}, Ls = {L[1], L[0]};
    u16x2 thr = __builtin_elementwise_max(__builtin_elementwise_min(H, Hs), __builtin_elementwise_max(L, Ls));
    unsigned T = ksgn - __builtin_bit_cast(unsigned, thr);
    unsigned t0 = m0 + T, t1 = m1 + T;
    s16x2 k0 = __builtin_bit_cast(s16x2, t0) >> 15, k1 = __builtin_bit_cast(s16x2, t1) >> 15;
    r0 = d0 & __builtin_bit_cast(unsigned, k0);
    r1 = d1 & __builtin_bit_cast(unsigned, k1);
}

DEVI unsigned cvt_pk(float a, float b) {
    f32x2 v = {a, b};
    return __builtin_bit_cast(unsigned, __builtin_convertvector(v, bf16x2_t));
}

template <bool BT> struct Cfg {
    static constexpr int RA = BT ? 6 : 4;
    static constexpr int MULT = BT ? 4 : 3;
    static constexpr int SLOT = MULT * 8 * KB;
    static constexpr int NB = BT ? 3 : 2;
    static constexpr int BSTEP = BT ? 64 * KB : 72 * KB;
    static constexpr int PBASE = 144 * KB;
    static constexpr int slot(int s) {
        if (BT) { int c = (s + 6) % 5; return c * SLOT; }
        return (s % 6) * SLOT;
    }
    static constexpr int late(int v) {
        if (BT) return (v >> 2) * SLOT + (v & 3) * KB;
        if (v < 2) return slot(8) + v * KB;
        return slot(3 + (v - 2) / 3) + ((v - 2) % 3) * KB;
    }
};

template <bool BT> struct Ctx {
    using C = Cfg<BT>;
    LDS3 char* lds;
    SrdX srdx; SrdW srdw;
    __amdgpu_buffer_rsrc_t srdo;
    unsigned wb;
    unsigned vw0, vw1;
    unsigned vx0, vx1, vx2;
    unsigned vlate;
    unsigned h;
    unsigned kabs, ksgn;
    unsigned wrd[C::NB][2];
    unsigned wrd2[2];
    unsigned xrd[C::NB][2];
    unsigned vout;
    u32x2 wraw[2][4];
    bf16x8 wf[9][2];
    bf16x8 xf[2][6];
    f32x4 acc[C::RA][2];
    f32x4 pacc[2][2][2];
    f32x4 bias[2];
};

template <bool BT, int S, int BUF> DEVI void read_wraw(Ctx<BT>& c) {
    using C = Cfg<BT>;
#pragma unroll
    for (int t = 0; t < 2; ++t)
#pragma unroll
        for (int e = 0; e < 2; ++e) {
            unsigned off;
            if (!BT && S == 0) off = c.wrd2[t] + e * 2 * KB;
            else {
                constexpr int sl = C::slot(S - 1), b = sl / C::BSTEP;
                off = c.wrd[b][t] + (sl - b * C::BSTEP) + e * (BT ? 512 : 3 * KB);
            }
            s16x4 v = __builtin_amdgcn_ds_read_tr16_b64_v4i16((LDS3 s16x4*)(c.lds + off));
            c.wraw[BUF][t * 2 + e] = __builtin_bit_cast(u32x2, v);
        }
}
template <bool BT, int S, int BUF, int T> DEVI void prune_w(Ctx<BT>& c) {
    u32x4 r;
    unsigned a, b;
    prune4(c.wraw[BUF][T * 2][0], c.wraw[BUF][T * 2][1], a, b, c.kabs, c.ksgn); r[0] = a; r[1] = b;
    prune4(c.wraw[BUF][T * 2 + 1][0], c.wraw[BUF][T * 2 + 1][1], a, b, c.kabs, c.ksgn); r[2] = a; r[3] = b;
    c.wf[S][T] = __builtin_bit_cast(bf16x8, r);
}
template <bool BT, int S, int BUF> DEVI void read_xa(Ctx<BT>& c) {
    using C = Cfg<BT>;
    constexpr int sl = C::slot(S), b = sl / C::BSTEP;
#pragma unroll
    for (int tl = 0; tl < C::RA; ++tl) {
        unsigned off = c.xrd[b][tl & 1] + (sl - b * C::BSTEP) + (BT ? (1 + (tl >> 1)) * KB : ((tl >> 1) * 12 + 2) * KB);
        c.xf[BUF][tl] = *(const LDS3 bf16x8*)(c.lds + off);
    }
}
template <bool BT, int S, int BUF, int T0, int T1> DEVI void mma_a(Ctx<BT>& c) {
#pragma unroll
    for (int tl = T0; tl < T1; ++tl)
#pragma unroll
        for (int t = 0; t < 2; ++t)
            c.acc[tl][t] = __builtin_amdgcn_mfma_f32_16x16x32_bf16(c.wf[S][t], c.xf[BUF][tl], S == 0 ? c.bias[t] : c.acc[tl][t], 0, 0, 0);
}
template <bool BT, int P, int S, int IDX, int BUF> DEVI void read_xp1(Ctx<BT>& c) {
    using C = Cfg<BT>;
    constexpr int un = 9 * P + S;
    constexpr int lo = BT ? C::late(S) : C::late(un >> 1);
    constexpr int b = lo / C::BSTEP;
    constexpr int rest = lo - b * C::BSTEP + (BT ? 0 : (un & 1) * 12 * KB);
    c.xf[BUF][IDX * 2 + 0] = *(const LDS3 bf16x8*)(c.lds + c.xrd[b][0] + rest);
    c.xf[BUF][IDX * 2 + 1] = *(const LDS3 bf16x8*)(c.lds + c.xrd[b][1] + rest);
}
template <bool BT, int P, int B, int BUF> DEVI void read_xp(Ctx<BT>& c) {
    read_xp1<BT, P, 3 * B, 0, BUF>(c); read_xp1<BT, P, 3 * B + 1, 1, BUF>(c); read_xp1<BT, P, 3 * B + 2, 2, BUF>(c);
}
template <bool BT, int P, int B, int BUF, int I0 = 0, int I1 = 3> DEVI void mma_p(Ctx<BT>& c) {
#pragma unroll
    for (int i = I0; i < I1; ++i)
#pragma unroll
        for (int par = 0; par < 2; ++par)
#pragma unroll
            for (int t = 0; t < 2; ++t)
                c.pacc[P & 1][par][t] = __builtin_amdgcn_mfma_f32_16x16x32_bf16(c.wf[3 * B + i][t], c.xf[BUF][i * 2 + par],
                                                                                (B == 0 && i == 0) ? c.bias[t] : c.pacc[P & 1][par][t], 0, 0, 0);
}
template <bool BT> DEVI void ex_write(Ctx<BT>& c, unsigned a0, unsigned a1, const f32x4& v0, const f32x4& v1) {
    *(LDS3 f32x4*)(c.lds + a0) = v0;
    *(LDS3 f32x4*)(c.lds + a1) = v1;
}
struct Ex { f32x4 p0, p1; };
template <bool BT> DEVI Ex ex_read(Ctx<BT>& c, unsigned a0, unsigned a1) {
    Ex e;
    e.p0 = *(const LDS3 f32x4*)(c.lds + a0);
    e.p1 = *(const LDS3 f32x4*)(c.lds + a1);
    return e;
}
DEVI float addf(float a, float b) { float r; asm("v_add_f32 %0, %1, %2" : "=v"(r) : "v"(a), "v"(b)); return r; }
template <bool BT, int TL> DEVI void finalize(Ctx<BT>& c, const Ex& e, const f32x4& v0, const f32x4& v1) {
    f32x4 s0 = v0 + e.p0, s1 = v1 + e.p1;
    unsigned q00 = cvt_pk(s0[0], s0[1]), q01 = cvt_pk(s0[2], s0[3]), q10 = cvt_pk(s1[0], s1[1]), q11 = cvt_pk(s1[2], s1[3]);
    auto x0 = __builtin_amdgcn_permlane16_swap(q00, q10, false, false);
    auto x1 = __builtin_amdgcn_permlane16_swap(q01, q11, false, false);
    u32x4 o = {x0[0], x1[0], x0[1], x1[1]};
    __builtin_amdgcn_raw_buffer_store_b128(o, c.srdo, c.vout, TL * (BT ? 32 : 16) * ROWB, STORE_AUX);
}

template <bool BT, int V> DEVI unsigned late_soff(Ctx<BT>& c) {
    constexpr int u = V < 2 ? V : (V - 2) % 3;
    constexpr int u0 = 2 * V, u1 = 2 * V + 1;
    constexpr int S0 = (64 + 32 * (u0 / 9)) * ROWB + (u0 % 9) * 128 - u * KB;
    constexpr int S1 = (64 + 32 * (u1 / 9)) * ROWB + (u1 % 9) * 128 - u * KB;
    return S0 + c.h * (S1 - S0);
}

template <bool BT, int N> DEVI void issue(Ctx<BT>& c) {
    using C = Cfg<BT>;
    constexpr int WCH = 64 * ROWB;
    if constexpr (BT) {
        if constexpr (N == 0) dma_set<C::slot(-1), 0>(c.wb, c.vw0, c.srdw, 0);
        else if constexpr (N <= 32) {
            constexpr int s = (N - 1) / 4, u = (N - 1) % 4;
            if constexpr (u == 0) dma_set<C::slot(s), 0>(c.wb, c.vw0, c.srdw, (s + 1) * WCH);
            else dma_nxt<u>(u == 1 ? c.vx0 : (u == 2 ? c.vx1 : c.vx2), c.srdx, s * 128);
        } else if constexpr (N == 33) dma_set<C::slot(8), 1>(c.wb, c.vx0, c.srdx, 8 * 128);
        else if constexpr (N <= 35) dma_nxt<N - 32>(N == 34 ? c.vx1 : c.vx2, c.srdx, 8 * 128);
        else if constexpr (N <= 44) {
            constexpr int v = N - 36, so = 192 * ROWB + v * 128 - (v & 3) * KB;
            if constexpr ((v & 3) == 0) dma_set<C::late(v), 0>(c.wb, c.vlate, c.srdx, so);
            else dma_nxt<(v & 3)>(c.vlate, c.srdx, so);
        }
    } else {
        if constexpr (N == 0) dma_set<C::PBASE, 0>(c.wb - (c.wb / 3072) * 1024, c.vw0, c.srdw, 0);
        else if constexpr (N == 1) dma_nxt<1>(c.vw1, c.srdw, 0);
        else if constexpr (N <= 25) {
            constexpr int s = (N - 2) / 3, u = (N - 2) % 3;
            if constexpr (u == 0) dma_set<C::slot(s), 0>(c.wb, c.vw0, c.srdw, (s + 1) * WCH);
            else if constexpr (u == 1) dma_nxt<1>(c.vw1, c.srdw, (s + 1) * WCH);
            else dma_nxt<2>(c.vx0, c.srdx, s * 128);
        } else if constexpr (N == 26) dma_set<C::slot(8), 0>(c.wb, c.vlate, c.srdx, late_soff<BT, 0>(c));
        else if constexpr (N == 27) dma_nxt<1>(c.vlate, c.srdx, late_soff<BT, 1>(c));
        else if constexpr (N == 28) dma_nxt<2>(c.vx0, c.srdx, 8 * 128);
        else if constexpr (N <= 40) {
            constexpr int v = N - 27, u = (v - 2) % 3;
            if constexpr (u == 0) dma_set<C::late(v), 0>(c.wb, c.vlate, c.srdx, late_soff<BT, v>(c));
            else dma_nxt<u>(c.vlate, c.srdx, late_soff<BT, v>(c));
        }
    }
}
template <bool BT, int N0, int N1> DEVI void issue_range(Ctx<BT>& c) {
    if constexpr (N0 < N1) { issue<BT, N0>(c); issue_range<BT, N0 + 1, N1>(c); }
}
template <bool BT> constexpr int issued(int s) {
    if (BT) { constexpr int t[11] = {9, 13, 17, 21, 25, 29, 33, 36, 40, 44, 45}; return t[s + 1]; }
    else    { constexpr int t[11] = {8, 11, 15, 19, 23, 26, 29, 32, 35, 38, 41}; return t[s + 1]; }
}
template <bool BT> constexpr int need(int s) {
    if (BT) return s < 0 ? 1 : (s < 8 ? 1 + 4 * (s + 1) : 36);
    return s < 0 ? 2 : 2 + 3 * (s + 1);
}

template <bool BT, int S> DEVI void stage_a(Ctx<BT>& c) {
    using C = Cfg<BT>;
    constexpr int N0 = issued<BT>(S), N1 = issued<BT>(S + 1);
    constexpr int H = C::RA / 2;
    ST(3 + 2 * S);
    wait_bar<N0 - need<BT>(S)>();
    ST(4 + 2 * S);
    if constexpr (S < 8) read_wraw<BT, S + 1, (S + 1) & 1>(c);
    read_xa<BT, S, S & 1>(c);
    SB();
    if constexpr (N0 < N1) issue<BT, N0>(c);
    SB();
    prune_w<BT, S, S & 1, 0>(c);
    if constexpr (S > 0) mma_a<BT, S - 1, (S - 1) & 1, 0, 1>(c);
    SB();
    if constexpr (N0 + 1 < N1) issue<BT, N0 + 1>(c);
    SB();
    if constexpr (S > 0) mma_a<BT, S - 1, (S - 1) & 1, 1, H>(c);
    SB();
    if constexpr (N0 + 2 < N1) issue<BT, N0 + 2>(c);
    SB();
    prune_w<BT, S, S & 1, 1>(c);
    if constexpr (S > 0) mma_a<BT, S - 1, (S - 1) & 1, H, H + 1>(c);
    SB();
    if constexpr (N0 + 3 < N1) issue<BT, N0 + 3>(c);
    SB();
    if constexpr (S > 0) mma_a<BT, S - 1, (S - 1) & 1, H + 1, C::RA>(c);
}

template <bool BT> DEVI void body(const uint16_t* x, const uint16_t* w, const uint16_t* bias, uint16_t* out,
                                  LDS3 char* lds, int row0, int nrows, int col0, unsigned* wsp) {
    using C = Cfg<BT>;
    Ctx<BT> c;
    c.lds = lds;
    ST(0);
    const unsigned long long rt0 = __builtin_amdgcn_s_memrealtime(); (void)rt0;
#define PAD(T) do { SB(); while (__builtin_amdgcn_s_memrealtime() - rt0 < (T)) __builtin_amdgcn_s_sleep(1); SB(); } while (0)
    const int lane = threadIdx.x & 63;
    const int wv = __builtin_amdgcn_readfirstlane(threadIdx.x >> 6);
    const int h = wv >> 2, cg = wv & 3;
    const int cc = BT ? (cg & 1) : cg, sg = BT ? (cg >> 1) : 0;
    c.h = h;
    c.srdx.r = make_srd((const char*)x + (size_t)row0 * ROWB - BIASB, nrows * ROWB + BIASB);
    c.srdw.r = make_srd((const char*)w - BIASB, 576 * ROWB + BIASB);
    c.srdo = __builtin_amdgcn_make_buffer_rsrc((void*)(out + (size_t)row0 * 576), 0, nrows * ROWB, 0x00020000);
    c.wb = wv * C::MULT * KB;
    const int xlane = (lane >> 3) * ROWB + (((lane & 7) ^ (4 * (wv & 1) + (lane >> 4))) * 16) + BIASB;
    if constexpr (BT) {
        const int q1 = (lane >> 3) & 3, g = (q1 >> 1) | ((wv & 1) << 1);
        c.vw0 = (8 * wv + (lane >> 3)) * ROWB + col0 * 2 + (((lane & 7) ^ (g << 1)) * 16) + BIASB;
        c.vw1 = 0;
        c.vx0 = 8 * wv * ROWB + xlane - KB;
        c.vx1 = c.vx0 + 64 * ROWB - KB;
        c.vx2 = c.vx1 + 64 * ROWB - KB;
        c.vlate = 8 * wv * ROWB + xlane;
    } else {
        const int g = (lane >> 4) | (((wv >> 1) & 1) << 2);
        c.vw0 = (4 * wv + (lane >> 4)) * ROWB + col0 * 2 + (((lane & 15) ^ (g << 1)) * 16) + BIASB;
        c.vw1 = c.vw0 + 32 * ROWB - KB;
        c.vx0 = 8 * wv * ROWB + xlane - 2 * KB;
        c.vx1 = c.vx2 = 0;
        c.vlate = 8 * (wv & 3) * ROWB + xlane;
    }
    issue_range<BT, 0, need<BT>(0)>(c);
    const int q = lane >> 4, gm = lane & 15;
    u32x2 braw[2];
    {
        const uint16_t* bp = bias + col0 + 32 * cc + 4 * q;
        asm volatile("global_load_dwordx2 %0, %2, off\n\tglobal_load_dwordx2 %1, %2, off offset:32"
                     : "=&v"(braw[0]), "=&v"(braw[1]) : "v"(bp) : "memory");
    }
    issue_range<BT, need<BT>(0), issued<BT>(-1)>(c);
    unsigned exw, exr;
    c.kabs = opaque_s(0x7fff7fffu);
    c.ksgn = opaque_s(0x80008000u);
    {
        const int q1 = gm >> 2, p = gm & 3;
#pragma unroll
        for (int t = 0; t < 2; ++t) {
            unsigned o;
            if constexpr (BT) {
                const int g = (q1 >> 1) | ((q & 1) << 1);
                o = 4 * (4 * h + q) * KB + q1 * 128 + (((2 * cc + t) ^ g) * 32) + 8 * p;
                c.wrd2[t] = 0;
            } else {
                const int g = q1 | ((q & 1) << 2);
                const int in = q1 * 256 + (((2 * cc + t) ^ g) * 32) + 8 * p;
                o = (6 * q + h) * KB + in;
                c.wrd2[t] = C::PBASE + (4 * q + h) * KB + in;
            }
#pragma unroll
            for (int b = 0; b < C::NB; ++b) c.wrd[b][t] = opaque_v(o + b * C::BSTEP);
        }
        const int j = gm;
        const int in = (j & 7) * 128 + (((4 * h + q) ^ (j >> 1)) * 16);
#pragma unroll
        for (int par = 0; par < 2; ++par) {
            unsigned o;
            if constexpr (BT) o = 4 * KB * (4 * (par ^ h) + 2 * sg + (j >> 3)) + in;
            else o = 3 * KB * (2 * (par ^ h) + (j >> 3)) + in;
#pragma unroll
            for (int b = 0; b < C::NB; ++b) c.xrd[b][par] = opaque_v(o + b * C::BSTEP);
        }
        exw = wv * 2 * KB + lane * 16;
        exr = (wv ^ 4) * 2 * KB + lane * 16;
        const int trow = BT ? (32 * h + 16 * sg + j) : (16 * h + j);
        c.vout = trow * ROWB + (col0 + 32 * cc + 16 * (q & 1) + 8 * (q >> 1)) * 2;
    }
    ST(1);
    wait_bar<issued<BT>(-1) + 2 - need<BT>(-1)>();
    ST(2);
    PAD(PAD1);
    read_wraw<BT, 0, 0>(c);
    SB();
    issue_range<BT, issued<BT>(-1), issued<BT>(0)>(c);
    SB();
    asm volatile("s_waitcnt vmcnt(%2)" : "+v"(braw[0]), "+v"(braw[1]) : "n"(issued<BT>(0) - need<BT>(0)) : "memory");
    SB();
#pragma unroll
    for (int t = 0; t < 2; ++t) {
        unsigned b0 = h ? 0u : braw[t][0], b1 = h ? 0u : braw[t][1];
        c.bias[t][0] = __builtin_bit_cast(float, b0 << 16);
        c.bias[t][1] = __builtin_bit_cast(float, b0 & 0xffff0000u);
        c.bias[t][2] = __builtin_bit_cast(float, b1 << 16);
        c.bias[t][3] = __builtin_bit_cast(float, b1 & 0xffff0000u);
    }
    stage_a<BT, 0>(c); stage_a<BT, 1>(c); stage_a<BT, 2>(c); stage_a<BT, 3>(c); stage_a<BT, 4>(c);
    stage_a<BT, 5>(c); stage_a<BT, 6>(c); stage_a<BT, 7>(c); stage_a<BT, 8>(c);

    if constexpr (BT) {
        constexpr int EA = 3 * C::SLOT;
        constexpr int EP = 3 * C::SLOT + 48 * KB;
        const unsigned w0 = opaque_v(exw + EA), r0 = opaque_v(exr + EA);
        wait_bar<3>();
        read_xp<BT, 0, 0, 1>(c);
        SB();
        mma_a<BT, 8, 0, 0, 6>(c);
        SB();
#pragma unroll
        for (int m = 0; m < 3; ++m) ex_write(c, w0 + m * 16 * KB, w0 + m * 16 * KB + KB, c.acc[2 * m + 1][0], c.acc[2 * m + 1][1]);
        read_xp<BT, 0, 1, 0>(c);
        SB();
        mma_p<BT, 0, 0, 1>(c);
        wait_bar<0>();
        Ex e0 = ex_read(c, r0, r0 + KB), e1 = ex_read(c, r0 + 16 * KB, r0 + 17 * KB), e2 = ex_read(c, r0 + 32 * KB, r0 + 33 * KB);
        read_xp<BT, 0, 2, 1>(c);
        SB();
        mma_p<BT, 0, 1, 0, 0, 1>(c);
        SB();
        finalize<BT, 0>(c, e0, c.acc[0][0], c.acc[0][1]);
        finalize<BT, 2>(c, e1, c.acc[2][0], c.acc[2][1]);
        finalize<BT, 4>(c, e2, c.acc[4][0], c.acc[4][1]);
        mma_p<BT, 0, 1, 0, 1, 3>(c);
        SB();
        mma_p<BT, 0, 2, 1>(c);
        ex_write(c, w0 + 48 * KB, w0 + 49 * KB, c.pacc[0][1][0], c.pacc[0][1][1]);
        lgkm_bar();
        Ex e3 = ex_read(c, r0 + 48 * KB, r0 + 49 * KB);
        finalize<BT, 6>(c, e3, c.pacc[0][0][0], c.pacc[0][0][1]);
        (void)EP;
    } else {
        const unsigned w0 = opaque_v(exw + C::PBASE), r0 = opaque_v(exr + C::PBASE);
        const unsigned w1 = opaque_v(exw + C::slot(1)), r1 = opaque_v(exr + C::slot(1));
        const unsigned e2w0 = C::slot(1) + 16 * KB + wv * KB + lane * 16, e2w1 = C::slot(2) + (3 * wv + 2) * KB + lane * 16;
        const unsigned e2r0 = C::slot(1) + 16 * KB + (wv ^ 4) * KB + lane * 16, e2r1 = C::slot(2) + (3 * (wv ^ 4) + 2) * KB + lane * 16;
        ST(21); wait_bar<11>(); ST(22);
        read_xp<BT, 0, 0, 1>(c);
        SB();
        mma_a<BT, 8, 0, 0, 4>(c);
        SB();
        ex_write(c, w0, w0 + KB, c.acc[1][0], c.acc[1][1]);
        ex_write(c, w1, w1 + KB, c.acc[3][0], c.acc[3][1]);
        read_xp<BT, 0, 1, 0>(c);
        SB();
        mma_p<BT, 0, 0, 1>(c);
        ST(23); wait_bar<9>(); ST(24);
        Ex ea = ex_read(c, r0, r0 + KB), eb = ex_read(c, r1, r1 + KB);
        read_xp<BT, 0, 2, 1>(c);
        SB();
        mma_p<BT, 0, 1, 0, 0, 1>(c);
        SB();
        finalize<BT, 0>(c, ea, c.acc[0][0], c.acc[0][1]);
        finalize<BT, 2>(c, eb, c.acc[2][0], c.acc[2][1]);
        mma_p<BT, 0, 1, 0, 1, 3>(c);
        ST(25); wait_bar<8>(); ST(26);
        read_xp<BT, 1, 0, 0>(c);
        SB();
        mma_p<BT, 0, 2, 1>(c);
        SB();
        read_xp<BT, 1, 1, 1>(c);
        SB();
        mma_p<BT, 1, 0, 0, 0, 1>(c);
        SB();
        ex_write(c, e2w0, e2w1, c.pacc[0][1][0], c.pacc[0][1][1]);
        SB();
        mma_p<BT, 1, 0, 0, 1, 3>(c);
        ST(27); wait_bar<7>(); ST(28);
        ea = ex_read(c, e2r0, e2r1);
        read_xp<BT, 1, 2, 0>(c);
        SB();
        mma_p<BT, 1, 1, 1, 0, 1>(c);
        SB();
        finalize<BT, 4>(c, ea, c.pacc[0][0][0], c.pacc[0][0][1]);
        mma_p<BT, 1, 1, 1, 1, 3>(c);
        if (nrows > 128) {
            ST(29); wait_bar<5>(); ST(30);
            read_xp<BT, 2, 0, 1>(c);
            SB();
            mma_p<BT, 1, 2, 0>(c);
            SB();
            read_xp<BT, 2, 1, 0>(c);
            SB();
            mma_p<BT, 2, 0, 1, 0, 1>(c);
            SB();
            ex_write(c, w0, w0 + KB, c.pacc[1][1][0], c.pacc[1][1][1]);
            SB();
            mma_p<BT, 2, 0, 1, 1, 3>(c);
            ST(31); wait_bar<3>(); ST(32);
            ea = ex_read(c, r0, r0 + KB);
            read_xp<BT, 2, 2, 1>(c);
            SB();
            mma_p<BT, 2, 1, 0, 0, 1>(c);
            SB();
            finalize<BT, 6>(c, ea, c.pacc[1][0][0], c.pacc[1][0][1]);
            mma_p<BT, 2, 1, 0, 1, 3>(c);
            SB();
            mma_p<BT, 2, 2, 1>(c);
            ex_write(c, w1, w1 + KB, c.pacc[0][1][0], c.pacc[0][1][1]);
            ST(33); lgkm_bar(); ST(34);
            ea = ex_read(c, r1, r1 + KB);
            finalize<BT, 8>(c, ea, c.pacc[0][0][0], c.pacc[0][0][1]);
        } else {
            SB();
            mma_p<BT, 1, 2, 0>(c);
            ex_write(c, w0, w0 + KB, c.pacc[1][1][0], c.pacc[1][1][1]);
            lgkm_bar();
            ea = ex_read(c, r0, r0 + KB);
            finalize<BT, 6>(c, ea, c.pacc[1][0][0], c.pacc[1][0][1]);
            asm volatile("s_waitcnt vmcnt(0)" ::: "memory");
        }
    }
}

__global__ __launch_bounds__(512, 2) void sparse24_gemm_kernel(const uint16_t* __restrict__ x, const uint16_t* __restrict__ w,
                                                               const uint16_t* __restrict__ bias, uint16_t* __restrict__ out, unsigned* wsp) {
    __shared__ __attribute__((aligned(1024))) char smem[163840];
    LDS3 char* lds = (LDS3 char*)smem;
    const int bid = blockIdx.x, xcd = bid & 7, j = bid >> 3;
    if (j < 28) {
        const int rbl = j >> 2, cb = j & 3;
        const int r0 = rbl < 4 ? rbl * 160 : 640 + (rbl - 4) * 128;
        body<false>(x, w, bias, out, lds, xcd * 1024 + r0, rbl < 4 ? 160 : 128, cb * 128, wsp);
    } else {
        body<true>(x, w, bias, out, lds, xcd * 1024 + (j - 28) * 256, 256, 512, wsp);
    }
}

extern "C" void kernel_launch(void* const* d_in, const int* in_sizes, int n_in, void* d_out, int out_size, void* d_ws,
                              size_t ws_size, hipStream_t stream) {
    sparse24_gemm_kernel<<<256, 512, 0, stream>>>((const uint16_t*)d_in[0], (const uint16_t*)d_in[1], (const uint16_t*)d_in[2],
                                                  (uint16_t*)d_out, (unsigned*)d_ws);
}
```

```cpp
#define GRID 576
#include <hip/hip_runtime.h>
#include <stdint.h>
typedef __attribute__((ext_vector_type(8))) short bf16x8;
typedef __attribute__((ext_vector_type(2))) short s16x2;
typedef __attribute__((ext_vector_type(2))) unsigned short u16x2;
typedef __attribute__((ext_vector_type(4))) float f32x4;
typedef __attribute__((ext_vector_type(4))) unsigned u32x4;
typedef __attribute__((ext_vector_type(2))) __bf16 bf16x2_t;
typedef __attribute__((ext_vector_type(2))) float f32x2;
#define NT 256
#define LDSB 0
__device__ __forceinline__ void prune4(unsigned d0, unsigned d1, unsigned& r0, unsigned& r1) {
    unsigned m0 = d0 & 0x7fff7fffu, m1 = d1 & 0x7fff7fffu;
    u16x2 a = __builtin_bit_cast(u16x2, m0), b = __builtin_bit_cast(u16x2, m1);
    u16x2 H = __builtin_elementwise_max(a, b), L = __builtin_elementwise_min(a, b);
    u16x2 Hs = {H[1], H[0]}, Ls = {L[1], L[0]};
    u16x2 thr = __builtin_elementwise_max(__builtin_elementwise_min(H, Hs), __builtin_elementwise_max(L, Ls));
    unsigned T = 0x80008000u - __builtin_bit_cast(unsigned, thr);
    unsigned t0 = m0 + T, t1 = m1 + T;
    s16x2 k0 = __builtin_bit_cast(s16x2, t0) >> 15, k1 = __builtin_bit_cast(s16x2, t1) >> 15;
    r0 = d0 & __builtin_bit_cast(unsigned, k0);
    r1 = d1 & __builtin_bit_cast(unsigned, k1);
}
__device__ __forceinline__ unsigned cvt_pk(float a, float b) { f32x2 v = {a, b}; return __builtin_bit_cast(unsigned, __builtin_convertvector(v, bf16x2_t)); }
__global__ __launch_bounds__(NT) void simple_kernel(const uint16_t* __restrict__ x, const uint16_t* __restrict__ w, const uint16_t* __restrict__ bias, uint16_t* __restrict__ out) {
    const unsigned long long rt0 = __builtin_amdgcn_s_memrealtime();
    const int lane = threadIdx.x & 63, q = lane >> 4, g = lane & 15;
    const int nw = GRID * (NT / 64);
    for (int task = blockIdx.x * (NT / 64) + (threadIdx.x >> 6); task < 2304; task += nw) {
        const int cgp = task % 18, rb = task / 18;
        f32x4 acc[4][2];
        for (int t = 0; t < 2; ++t) for (int r = 0; r < 4; ++r) { float bv = __builtin_bit_cast(float, (unsigned)bias[cgp * 32 + 8 * q + 2 * r + t] << 16); for (int m = 0; m < 4; ++m) acc[m][t][r] = bv; }
        const uint16_t* wp = w + (size_t)(8 * q) * 576 + cgp * 32 + 2 * g;
        const uint16_t* xp = x + (size_t)(rb * 64 + g) * 576 + 8 * q;
#pragma unroll 1
        for (int k = 0; k < 576; k += 32) {
            unsigned d[8];
            for (int i = 0; i < 8; ++i) d[i] = *(const unsigned*)(wp + (size_t)(k + i) * 576);
            u32x4 f[2];
            for (int e = 0; e < 2; ++e) {
                unsigned lo0 = __builtin_amdgcn_perm(d[4 * e + 1], d[4 * e], 0x05040100), lo1 = __builtin_amdgcn_perm(d[4 * e + 3], d[4 * e + 2], 0x05040100);
                unsigned hi0 = __builtin_amdgcn_perm(d[4 * e + 1], d[4 * e], 0x07060302), hi1 = __builtin_amdgcn_perm(d[4 * e + 3], d[4 * e + 2], 0x07060302);
                unsigned a, b;
                prune4(lo0, lo1, a, b); f[0][2 * e] = a; f[0][2 * e + 1] = b;
                prune4(hi0, hi1, a, b); f[1][2 * e] = a; f[1][2 * e + 1] = b;
            }
            for (int m = 0; m < 4; ++m) {
                bf16x8 xf = *(const bf16x8*)(xp + (size_t)m * 16 * 576 + k);
                for (int t = 0; t < 2; ++t) acc[m][t] = __builtin_amdgcn_mfma_f32_16x16x32_bf16(__builtin_bit_cast(bf16x8, f[t]), xf, acc[m][t], 0, 0, 0);
            }
        }
        for (int m = 0; m < 4; ++m) {
            u32x4 o;
            for (int r = 0; r < 4; ++r) o[r] = cvt_pk(acc[m][0][r], acc[m][1][r]);
            *(u32x4*)(out + (size_t)(rb * 64 + m * 16 + g) * 576 + cgp * 32 + 8 * q) = o;
        }
    }
}
extern "C" void kernel_launch(void* const* d_in, const int* in_sizes, int n_in, void* d_out, int out_size, void* d_ws, size_t ws_size, hipStream_t stream) {
    simple_kernel<<<GRID, NT, 0, stream>>>((const uint16_t*)d_in[0], (const uint16_t*)d_in[1], (const uint16_t*)d_in[2], (uint16_t*)d_out);
}
```

```cpp
#include <hip/hip_runtime.h>
#include <stdint.h>

typedef __attribute__((ext_vector_type(8))) short bf16x8;
typedef __attribute__((ext_vector_type(4))) short s16x4;
typedef __attribute__((ext_vector_type(4))) float f32x4;
typedef __attribute__((ext_vector_type(16))) float f32x16;
typedef __attribute__((ext_vector_type(4))) unsigned u32x4;
typedef __attribute__((ext_vector_type(2))) unsigned u32x2;
typedef __attribute__((ext_vector_type(2))) __bf16 bf16x2_t;
typedef __attribute__((ext_vector_type(2))) float f32x2;
#define LDS3 __attribute__((address_space(3)))
#define DEVI __device__ __forceinline__

constexpr int STORE_AUX = 16;

constexpr int ROWB = 1152;
constexpr int KB = 1024;
constexpr int CODE_BYTES = 31232;
constexpr int BIASB = 4096;

#define SB() __builtin_amdgcn_sched_barrier(0)
#define MEMF() asm volatile("" ::: "memory")
constexpr int wenc(int vm, int lgkm) { return (vm & 15) | (7 << 4) | (lgkm << 8) | ((vm >> 4) << 14); }
template <int N> DEVI void wait_bar() {
    SB(); MEMF();
    __builtin_amdgcn_s_waitcnt(wenc(N, 0));
    __builtin_amdgcn_s_barrier();
    MEMF(); SB();
}
DEVI void lgkm_bar() { wait_bar<63>(); }
DEVI void bare_bar() { SB(); MEMF(); __builtin_amdgcn_s_barrier(); MEMF(); SB(); }

struct SrdX { u32x4 r; };
struct SrdW { u32x4 r; };
template <int C> DEVI void set_m0(unsigned wb) {
    asm volatile("s_add_u32 m0, %0, %1" ::"s"(wb), "n"(C) : "memory", "scc");
}
template <int U> DEVI void dma(unsigned v, SrdX r, unsigned so) {
    asm volatile("buffer_load_dwordx4 %0, %1, %2 offen offset:%3 lds" ::"v"(v), "s"(r.r), "s"(so), "n"(U * 1024) : "memory");
}
template <int U> DEVI void dma(unsigned v, SrdW r, unsigned so) {
    asm volatile("buffer_load_dwordx4 %0, %1, %2 offen offset:%3 lds" ::"v"(v), "s"(r.r), "s"(so), "n"(U * 1024) : "memory");
}
DEVI unsigned opaque_v(unsigned x) { asm volatile("" : "+v"(x)); return x; }
DEVI unsigned opaque_s(unsigned x) { asm volatile("" : "+s"(x)); return x; }

DEVI u32x4 make_srd(const void* p, unsigned bytes) {
    uint64_t a = (uint64_t)p;
    u32x4 r;
    r[0] = __builtin_amdgcn_readfirstlane((unsigned)a);
    r[1] = __builtin_amdgcn_readfirstlane((unsigned)(a >> 32) & 0xffffu);
    r[2] = __builtin_amdgcn_readfirstlane(bytes);
    r[3] = 0x00020000u;
    return r;
}

DEVI void prune8(unsigned d0, unsigned d1, unsigned e0, unsigned e1, unsigned& r0, unsigned& r1, unsigned& q0, unsigned& q1, unsigned kabs) {
    unsigned a, b, c, d, H, L, G, M;
    asm("v_and_b32 %4, %12, %8\n\t"
        "v_and_b32 %5, %12, %9\n\t"
        "v_and_b32 %6, %12, %10\n\t"
        "v_and_b32 %7, %12, %11\n\t"
        "v_pk_max_u16 %0, %4, %5\n\t"
        "v_pk_min_u16 %1, %4, %5\n\t"
        "v_pk_max_u16 %2, %6, %7\n\t"
        "v_pk_min_u16 %3, %6, %7\n\t"
        "v_pk_min_u16 %0, %0, %0 op_sel:[0,1] op_sel_hi:[1,0]\n\t"
        "v_pk_max_u16 %1, %1, %1 op_sel:[0,1] op_sel_hi:[1,0]\n\t"
        "v_pk_min_u16 %2, %2, %2 op_sel:[0,1] op_sel_hi:[1,0]\n\t"
        "v_pk_max_u16 %3, %3, %3 op_sel:[0,1] op_sel_hi:[1,0]\n\t"
        "v_pk_max_u16 %0, %0, %1\n\t"
        "v_pk_max_u16 %2, %2, %3\n\t"
        "v_pk_sub_i16 %4, %4, %0\n\t"
        "v_pk_sub_i16 %6, %6, %2\n\t"
        "v_pk_sub_i16 %5, %5, %0\n\t"
        "v_pk_sub_i16 %7, %7, %2\n\t"
        "v_pk_ashrrev_i16 %4, 15, %4 op_sel_hi:[0,1]\n\t"
        "v_pk_ashrrev_i16 %6, 15, %6 op_sel_hi:[0,1]\n\t"
        "v_pk_ashrrev_i16 %5, 15, %5 op_sel_hi:[0,1]\n\t"
        "v_pk_ashrrev_i16 %7, 15, %7 op_sel_hi:[0,1]\n\t"
        "v_bitop3_b32 %0, %8, %4, %8 bitop3:0x30\n\t"
        "v_bitop3_b32 %2, %10, %6, %10 bitop3:0x30\n\t"
        "v_bitop3_b32 %1, %9, %5, %9 bitop3:0x30\n\t"
        "v_bitop3_b32 %3, %11, %7, %11 bitop3:0x30"
        : "=&v"(H), "=&v"(L), "=&v"(G), "=&v"(M), "=&v"(a), "=&v"(b), "=&v"(c), "=&v"(d)
        : "v"(d0), "v"(d1), "v"(e0), "v"(e1), "s"(kabs));
    r0 = H; r1 = L; q0 = G; q1 = M;
}

DEVI unsigned cvt_pk(float a, float b) {
    f32x2 v = {a, b};
    return __builtin_bit_cast(unsigned, __builtin_convertvector(v, bf16x2_t));
}

constexpr int PG = 1;
template <bool BT> struct G {
    static constexpr int RA = BT ? 2 : 3;
    static constexpr int MULT = BT ? 3 : 4;
    static constexpr int SLOT = MULT * 8 * KB;
    static constexpr int BSTEP = BT ? 72 * KB : 64 * KB;
    static constexpr int LEAD = BT ? 5 : 4;
    static constexpr int NG = BT ? 14 : 11;
    static constexpr int slot(int g) { return BT ? (g < 0 ? 144 * KB : (g % 6) * SLOT) : ((g + 6) % 5) * SLOT; }
    static constexpr int late(int v) {
        if (BT) return v < 1 ? slot(8) : slot(3 + (v - 1) / 3 + (v >= 16 ? 1 : 0)) + ((v - 1) % 3) * KB;
        return (v >> 2) * SLOT + (v & 3) * KB;
    }
    static constexpr int tile(int tl) { return BT ? (1 + tl) * KB : (tl == 0 ? 2 * KB : (tl == 1 ? 18 * KB : 3 * KB)); }
    static constexpr int ex(int t, int q) {
        if (!BT) return 3 * SLOT + (t == 0 ? 1 : (t == 1 || t == 4 ? 0 : t)) * 16 * KB + q * KB;
        if (t & 1) return 144 * KB + q * KB;
        if (t == 0) return slot(2) + (q >> 1) * 12 * KB + (1 + (q & 1)) * KB;
        return (q & 1 ? slot(3) + 2 * KB : slot(2)) + (q >> 1) * 12 * KB;
    }
};

template <bool BT, int H> struct Ctx {
    LDS3 char* lds;
    SrdX srdx; SrdW srdw;
    __amdgpu_buffer_rsrc_t srdo;
    unsigned wb;
    unsigned wb0;
    unsigned vw, vw1, vx[2], vl[2];
    unsigned oso[4];
    unsigned xo[9], wo[9];
    unsigned kabs;
    unsigned wrd0, ex3, ex4;
    unsigned wrd[3], xrd[3][2];
    unsigned vout, tdummy, tdum2;
    u32x2 wraw[2][4];
    u32x4 wf[9][2];
    bf16x8 xf[2][6];
    f32x16 acc[5];
    f32x16 bias;
    f32x4 p[4];
    unsigned o[8];
};

template <bool BT, int H, int S, int BUF, int I0, int I1> DEVI void read_wraw(Ctx<BT, H>& c) {
    using g = G<BT>;
    constexpr int sl = g::slot(S - 1), b = sl / g::BSTEP;
#pragma unroll
    for (int i = I0; i < I1; ++i) {
        unsigned off;
        if constexpr (BT && S == 0) off = c.wrd0 + (i >> 1) * 4 * KB + (i & 1) * 512;
        else off = c.wrd[b] + (sl - b * g::BSTEP) + (i >> 1) * 2 * g::MULT * KB + (i & 1) * 512;
        c.wraw[BUF][i] = __builtin_bit_cast(u32x2, __builtin_amdgcn_ds_read_tr16_b64_v4i16((LDS3 s16x4*)(c.lds + off)));
    }
}
template <bool BT, int H, int S, int BUF, int KK> DEVI void prune_w(Ctx<BT, H>& c) {
    unsigned a0, b0, a1, b1;
    prune8(c.wraw[BUF][2 * KK][0], c.wraw[BUF][2 * KK][1], c.wraw[BUF][2 * KK + 1][0], c.wraw[BUF][2 * KK + 1][1], a0, b0, a1, b1, c.kabs);
    c.wf[S][KK] = (u32x4){a0, b0, a1, b1};
}
template <bool BT, int H, int S, int BUF, int I0, int I1> DEVI void read_xa(Ctx<BT, H>& c) {
    using g = G<BT>;
    constexpr int sl = g::slot(S), b = sl / g::BSTEP;
#pragma unroll
    for (int i = I0; i < I1; ++i)
        if (i < 2 * g::RA) c.xf[BUF][i] = *(const LDS3 bf16x8*)(c.lds + c.xrd[b][i / g::RA] + (sl - b * g::BSTEP) + g::tile(i % g::RA));
}
template <bool BT, int H, int S, int BUF, int I> DEVI void mma_a(Ctx<BT, H>& c) {
    constexpr int RA = G<BT>::RA;
    if constexpr (I < 2 * RA)
        c.acc[I % RA] = __builtin_amdgcn_mfma_f32_32x32x16_bf16(__builtin_bit_cast(bf16x8, c.wf[S][I / RA]), c.xf[BUF][I], (S == 0 && I < RA) ? c.bias : c.acc[I % RA], 0, 0, 0);
}
template <bool BT, int H, int P, int B, int BUF, int I> DEVI void read_xp(Ctx<BT, H>& c) {
    using g = G<BT>;
    constexpr int un = 9 * P + 3 * B + I / 2;
    constexpr int lo = BT ? g::late(un) : g::late(un >> 1) + (un & 1) * 16 * KB;
    constexpr int b = lo / g::BSTEP;
    c.xf[BUF][I] = *(const LDS3 bf16x8*)(c.lds + c.xrd[b][I & 1] + (lo - b * g::BSTEP));
}
template <bool BT, int H, int P, int B, int BUF, int I> DEVI void mma_p(Ctx<BT, H>& c) {
    f32x16& d = c.acc[G<BT>::RA + P];
    d = __builtin_amdgcn_mfma_f32_32x32x16_bf16(__builtin_bit_cast(bf16x8, c.wf[3 * B + I / 2][I & 1]), c.xf[BUF][I], (B == 0 && I == 0) ? c.bias : d, 0, 0, 0);
}
template <bool BT, int H, int T, int Q> DEVI unsigned ex_addr(Ctx<BT, H>& c) {
    constexpr int o = G<BT>::ex(T, Q);
    if constexpr (BT) { if constexpr (T & 1) return c.ex4 + (o - 144 * KB); else return c.ex3 + (o - G<BT>::slot(2)); }
    else return c.ex4 + (o - 3 * G<BT>::SLOT);
}
template <bool BT, int H, int T, int Q> DEVI void ex_write(Ctx<BT, H>& c) {
    const f32x16& a = c.acc[T];
    *(LDS3 f32x4*)(c.lds + ex_addr<BT, H, T, Q>(c)) = (f32x4){a[4 * Q], a[4 * Q + 1], a[4 * Q + 2], a[4 * Q + 3]};
}
template <bool BT, int H, int T, int Q> DEVI void ex_read(Ctx<BT, H>& c) { c.p[Q] = *(const LDS3 f32x4*)(c.lds + ex_addr<BT, H, T, Q>(c)); }
template <bool BT, int H, int T, int Q, int PQ> DEVI void fin_q(Ctx<BT, H>& c) {
    const f32x16& a = c.acc[T];
    c.o[2 * Q] = cvt_pk(a[4 * Q] + c.p[PQ][0], a[4 * Q + 1] + c.p[PQ][1]);
    c.o[2 * Q + 1] = cvt_pk(a[4 * Q + 2] + c.p[PQ][2], a[4 * Q + 3] + c.p[PQ][3]);
}
template <bool BT, int H, int I> DEVI void fin_swap(Ctx<BT, H>& c) {
    auto s = __builtin_amdgcn_permlane16_swap(c.o[I], c.o[4 + I], false, false);
    c.o[I] = s[0]; c.o[4 + I] = s[1];
}
template <bool BT, int H, int T, int HF> DEVI void fin_store(Ctx<BT, H>& c) {
    __builtin_amdgcn_raw_buffer_store_b128((u32x4){c.o[4 * HF], c.o[4 * HF + 1], c.o[4 * HF + 2], c.o[4 * HF + 3]}, c.srdo, c.vout,
                                           (BT ? c.oso[T & 3] : T * 32 * ROWB) + HF * 16 * ROWB, STORE_AUX);
}
template <bool BT, int H, int T, int R> DEVI void half_swap(Ctx<BT, H>& c) {
    f32x16& a = c.acc[T];
    const float lo = a[R], hi = a[R + 8];
    auto s = __builtin_amdgcn_permlane16_swap(__float_as_uint(lo), __float_as_uint(hi), false, false);
    const unsigned s0 = s[0], s1 = s[1];
    a[R] = __uint_as_float(s0);
    a[R + 8] = __uint_as_float(s1);
}

template <bool BT, int H> constexpr bool has_ld(int g, int u) {
    if (BT) return u < 3 && (g >= 9 ? 1 + 3 * (g - 9) + u <= 17 : (g >= 0 || u == 0));
    if (g >= 9) return 4 * (g - 9) + u <= 8;
    if (u < 2) return g < 8;
    return g >= 0 && (u == 2 || H == 0);
}
template <bool BT, int H> constexpr int gend(int g) {
    int n = 0;
    for (int i = -1; i <= g; ++i) for (int u = 0; u < 4; ++u) n += has_ld<BT, H>(i, u) ? 1 : 0;
    return n;
}
template <bool BT, int H, int GI> DEVI void grp_m0(Ctx<BT, H>& c) {
    using g = G<BT>;
    if constexpr (BT && GI < 0) set_m0<g::slot(GI)>(c.wb0);
    else if constexpr (GI <= 8) set_m0<g::slot(GI)>(c.wb);
    else if constexpr (GI <= g::NG) set_m0<g::late(BT ? 1 + 3 * (GI - 9) : 4 * (GI - 9))>(c.wb);
}
template <bool BT, int H, int GI, int U> DEVI void grp_ld(Ctx<BT, H>& c) {
    if constexpr (has_ld<BT, H>(GI, U)) {
        if constexpr (BT) {
            if constexpr (GI >= 9 || (GI == 8 && U == 0)) { constexpr int v = GI == 8 ? 0 : 1 + 3 * (GI - 9) + U; dma<U>(c.vl[v / 9], c.srdx, c.xo[v % 9] + (3 - U) * KB); }
            else if constexpr (U == 0) dma<0>(c.vw, c.srdw, c.wo[GI + 1]);
            else dma<U>(c.vx[U - 1], c.srdx, c.xo[GI]);
        } else if constexpr (GI >= 9) {
            constexpr int un = 2 * (4 * (GI - 9) + U) + H;
            dma<U>(c.vl[0], c.srdx, c.xo[un % 9] + ((96 + 32 * (un / 9)) * ROWB + (3 - U) * KB));
        } else if constexpr (U < 2) dma<U>(U == 0 ? c.vw : c.vw1, c.srdw, c.wo[GI + 1]);
        else dma<U>(c.vx[U - 2], c.srdx, c.xo[GI]);
    }
}
template <bool BT, int H, int GI> DEVI void grp_all(Ctx<BT, H>& c) {
    grp_m0<BT, H, GI>(c); asm volatile("s_nop 0");
    grp_ld<BT, H, GI, 0>(c); grp_ld<BT, H, GI, 1>(c); grp_ld<BT, H, GI, 2>(c); grp_ld<BT, H, GI, 3>(c);
    SB();
}

template <bool BT, int H, int S> DEVI void stage_a(Ctx<BT, H>& c) {
    constexpr int GI = S + G<BT>::LEAD, PB = (S - 1) & 1, CB = S & 1, NB = (S + 1) & 1;
#define MA(i) do { mma_a<BT, H, S - 1, PB, i>(c); SB(); } while (0)
#define RW(i) do { if constexpr (S < 8) read_wraw<BT, H, S + 1, NB, i, i + 2>(c); SB(); } while (0)
#define RX(i) do { read_xa<BT, H, S, CB, i, i + 2>(c); SB(); } while (0)
#define PR(i) do { prune_w<BT, H, S, CB, i>(c); SB(); } while (0)
#define DM(i) do { grp_ld<BT, H, GI, i>(c); SB(); } while (0)
    wait_bar<gend<BT, H>(GI - 1) - gend<BT, H>(S) + (S <= PG ? 1 : 0)>();
    if constexpr (S == 3) asm volatile("" :: "v"(c.tdummy));
    grp_m0<BT, H, GI>(c); SB();
    if constexpr (H == 0) { MA(0); PR(0); PR(1); RW(0); DM(0); RW(2); MA(1); RX(0); DM(1); MA(2); RX(2); MA(3); DM(2); RX(4); MA(4); DM(3); MA(5); }
    else { MA(0); DM(0); RW(0); MA(1); DM(1); RW(2); MA(2); DM(2); RX(0); MA(3); DM(3); RX(2); MA(4); RX(4); MA(5); PR(0); PR(1); }
#undef MA
#undef RW
#undef RX
#undef PR
#undef DM
}

template <bool BT, int H> DEVI void body(const uint16_t* x, const uint16_t* w, const uint16_t* bias, uint16_t* out, LDS3 char* lds,
                                         int cg, int row0, int nrows, int col0, int rot, uint64_t tiles) {
    Ctx<BT, H> c;
    c.lds = lds;
    asm volatile(".Lsp24_path_%0:" :: "n"(2 * BT + H));
    const int lane = threadIdx.x & 63, wv = 4 * H + cg;
    rot = opaque_s(rot);
    const int cc = BT ? (cg & 1) : cg, sg = BT ? (cg >> 1) : 0;
    c.srdx.r = make_srd((const char*)x + (size_t)row0 * ROWB - BIASB, nrows * ROWB + BIASB);
    c.srdw.r = make_srd((const char*)w - BIASB, 576 * ROWB + BIASB);
    c.srdo = __builtin_amdgcn_make_buffer_rsrc((void*)(out + (size_t)row0 * 576), 0, nrows * ROWB, 0x00020000);
    using g = G<BT>;
    c.wb0 = wv * 2 * KB;
    c.wb = wv * g::MULT * KB;
    const int rho = lane >> 3, hh = lane >> 5;
    c.vw = (8 * wv + rho) * ROWB + col0 * 2 + (((lane & 7) ^ (4 * ((rho >> 1) & 1))) * 16) + BIASB;
    c.vw1 = c.vw + 128 - KB;
    c.wo[0] = rot * (64 * ROWB);
    grp_all<BT, H, -1>(c);
    const int xlane = (8 * cg + rho) * ROWB + (((lane & 7) ^ (4 * (cg & 1) + (lane >> 4))) * 16) + BIASB;
    if constexpr (BT) {
#pragma unroll
        for (int t = 0; t < 2; ++t) {
            c.vx[t] = (int)((tiles >> (16 * t + 8 * H)) & 31) * 32 * ROWB + xlane - (1 + t) * KB;
            c.vl[t] = (int)((tiles >> (32 + 16 * t + 8 * H)) & 31) * 32 * ROWB + xlane - 3 * KB;
        }
#pragma unroll
        for (int t = 0; t < 4; ++t) c.oso[t] = (int)((tiles >> (16 * t + 8 * sg)) & 31) * 32 * ROWB;
    } else {
        c.vx[0] = 32 * H * ROWB + xlane - 2 * KB;
        c.vx[1] = 64 * ROWB + xlane - 3 * KB;
        c.vl[0] = xlane - 3 * KB;
    }
#pragma unroll
    for (int s = 0; s < 9; ++s) {
        const unsigned ch = s + rot >= 9 ? s + rot - 9 : s + rot;
        c.xo[s] = opaque_s(ch * 128);
        c.wo[s] = opaque_s(ch * (64 * ROWB));
    }
    bare_bar();
    grp_all<BT, H, 0>(c);
    u32x4 braw[2];
    {
        const uint16_t* bp = bias + col0 + 32 * cc + 8 * hh;
        asm volatile("global_load_dwordx4 %0, %2, off\n\tglobal_load_dwordx4 %1, %2, off offset:32"
                     : "=&v"(braw[0]), "=&v"(braw[1]) : "v"(bp) : "memory");
    }
    bare_bar();
    grp_all<BT, H, 1>(c);
    {
        const int st = wv >= 5 ? lane : 0;
        const char* tp;
        if (wv == 6) tp = (const char*)x + (size_t)row0 * ROWB + min(st * 65536, nrows * ROWB - 64);
        else if (wv == 5) tp = (const char*)w + min(st * 65536, 576 * ROWB - 64);
        else if (!BT && nrows == 128 && wv == 7) tp = (const char*)out + (size_t)(row0 & ~1023) * ROWB + min(st * 65536, 1024 * ROWB - 64);
        else tp = (const char*)w;
        asm volatile("global_load_dword %0, %1, off" : "=v"(c.tdummy) : "v"(tp) : "memory");
    }
    c.kabs = opaque_s(0x7fff7fffu);
    {
        const int gm = lane & 15, beta = (lane >> 4) & 1, q1 = gm >> 2, p = gm & 3, j = lane & 31;
        const int colb = 64 * (cc & 1) + 32 * beta + 8 * (2 * (p & 1) + (p >> 1));
        const unsigned win = (BT ? 0 : (cc >> 1)) * KB + q1 * 128 + (colb ^ (64 * (q1 >> 1)));
#pragma unroll
        for (int b = 0; b < (BT ? 2 : 3); ++b) c.wrd[b] = opaque_v(g::MULT * (4 * H + hh) * KB + win + b * g::BSTEP);
        c.wrd0 = g::slot(-1) + 2 * (4 * H + hh) * KB + win;
#pragma unroll
        for (int kk = 0; kk < 2; ++kk) {
            const unsigned xo = (j >> 3) * g::MULT * KB + sg * 12 * KB + (j & 7) * 128 + (((4 * H + 2 * kk + hh) ^ ((j >> 1) & 7)) * 16);
#pragma unroll
            for (int b = 0; b < (BT ? 2 : 3); ++b) c.xrd[b][kk] = opaque_v(xo + b * g::BSTEP);
        }
        c.ex4 = opaque_v((BT ? 144 * KB : 3 * g::SLOT) + cg * 4 * KB + lane * 16);
        if constexpr (BT) c.ex3 = opaque_v(g::slot(2) + cg * 3 * KB + lane * 16);
        c.vout = gm * ROWB + (col0 + 32 * cc + 16 * beta + 8 * hh) * 2;
    }
    wait_bar<gend<BT, H>(PG) + 3 - gend<BT, H>(-1)>();
    read_wraw<BT, H, 0, 0, 0, 4>(c);
    SB();
    grp_all<BT, H, 2>(c);
    prune_w<BT, H, 0, 0, 0>(c);
    SB();
    asm volatile("s_waitcnt vmcnt(%2)" : "+v"(braw[0]), "+v"(braw[1]) : "n"(gend<BT, H>(2) + 1 - gend<BT, H>(0)) : "memory");
    lgkm_bar();
    read_wraw<BT, H, 1, 1, 0, 4>(c);
    read_xa<BT, H, 0, 0, 0, 6>(c);
    SB();
    grp_all<BT, H, 3>(c);
    prune_w<BT, H, 0, 0, 1>(c);
    SB();
    grp_all<BT, H, 4>(c);
    if constexpr (BT) grp_all<BT, H, 5>(c);
#pragma unroll
    for (int i = 0; i < 8; ++i) {
        const unsigned b0 = H ? 0u : braw[i >> 2][i & 3];
        c.bias[2 * i + 0] = __uint_as_float(b0 << 16);
        c.bias[2 * i + 1] = __uint_as_float(b0 & 0xffff0000u);
    }
    stage_a<BT, H, 1>(c); stage_a<BT, H, 2>(c); stage_a<BT, H, 3>(c); stage_a<BT, H, 4>(c);
    stage_a<BT, H, 5>(c); stage_a<BT, H, 6>(c); stage_a<BT, H, 7>(c); stage_a<BT, H, 8>(c);

#define MA(i) do { mma_a<BT, H, 8, 0, i>(c); SB(); } while (0)
#define MP(P, B, BUF, i) do { mma_p<BT, H, P, B, BUF, i>(c); SB(); } while (0)
#define RP(P, B, BUF, i) do { read_xp<BT, H, P, B, BUF, i>(c); SB(); } while (0)
#define XW(T, Q) do { if constexpr (((T) & 1) != H) { ex_write<BT, H, T, Q>(c); SB(); } } while (0)
#define XR(T) do { if constexpr (((T) & 1) == H) { ex_read<BT, H, T, 0>(c); ex_read<BT, H, T, 2>(c); ex_read<BT, H, T, 1>(c); ex_read<BT, H, T, 3>(c); SB(); } } while (0)
#define FQ(T, Q) do { if constexpr (((T) & 1) == H) { fin_q<BT, H, T, Q, Q>(c); if constexpr ((Q) >= 2) { fin_swap<BT, H, 2 * ((Q) & 1)>(c); fin_swap<BT, H, 2 * ((Q) & 1) + 1>(c); } SB(); } } while (0)
#define FS(T, HF) do { if constexpr (((T) & 1) == H) { fin_store<BT, H, T, HF>(c); SB(); } } while (0)
#define GL(gi, u) do { grp_ld<BT, H, gi, u>(c); SB(); } while (0)
#define LW0() do { __builtin_amdgcn_s_waitcnt(wenc(63, 0)); SB(); } while (0)
#define BATCH(P, B, BUF, P2, B2, F0, F1, F2, F3, F4, F5) \
    LW0(); MP(P, B, BUF, 0); F0; RP(P2, B2, 1 - BUF, 0); RP(P2, B2, 1 - BUF, 1); MP(P, B, BUF, 1); F1; RP(P2, B2, 1 - BUF, 2); RP(P2, B2, 1 - BUF, 3); \
    MP(P, B, BUF, 2); F2; RP(P2, B2, 1 - BUF, 4); RP(P2, B2, 1 - BUF, 5); MP(P, B, BUF, 3); F3; MP(P, B, BUF, 4); F4; MP(P, B, BUF, 5); F5
#define LAST(P, B, BUF, F0, F1, F2, F3, F4, F5) \
    LW0(); MP(P, B, BUF, 0); F0; MP(P, B, BUF, 1); F1; MP(P, B, BUF, 2); F2; MP(P, B, BUF, 3); F3; MP(P, B, BUF, 4); F4; MP(P, B, BUF, 5); F5
#define NOP do {} while (0)
    constexpr int TK = H, TS = 1 - H;
    if constexpr (BT) {
        wait_bar<10>();
        MA(TS); RP(0, 0, 1, 0); RP(0, 0, 1, 1); MA(2 + TS); RP(0, 0, 1, 2); RP(0, 0, 1, 3); MA(TK); RP(0, 0, 1, 4); RP(0, 0, 1, 5); MA(2 + TK);
        BATCH(0, 0, 1, 0, 1, XW(TS, 0), XW(TS, 1), XW(TS, 2), XW(TS, 3), NOP, NOP);
        wait_bar<1>();
        grp_m0<BT, H, 14>(c); SB();
        BATCH(0, 1, 0, 0, 2, XR(TK), GL(14, 0), GL(14, 1), FQ(TK, 0), FQ(TK, 2), FQ(TK, 1));
        BATCH(0, 2, 1, 1, 0, FQ(TK, 3), NOP, FS(TK, 0), NOP, NOP, FS(TK, 1));
        BATCH(1, 0, 0, 1, 1, NOP, XW(2, 0), XW(2, 1), XW(2, 2), XW(2, 3), NOP);
        wait_bar<2>();
        BATCH(1, 1, 1, 1, 2, XR(2), NOP, NOP, FQ(2, 0), FQ(2, 2), FQ(2, 1));
        LAST(1, 2, 0, FQ(2, 3), FS(2, 0), NOP, NOP, FS(2, 1), NOP);
        XW(3, 0); XW(3, 1); XW(3, 2); XW(3, 3);
        lgkm_bar();
        XR(3); FQ(3, 0); FQ(3, 2); FQ(3, 1); FQ(3, 3); FS(3, 0); FS(3, 1);
    } else {
        wait_bar<6>();
        if constexpr (H == 0) { MA(1); RP(0, 0, 1, 0); RP(0, 0, 1, 1); MA(4); RP(0, 0, 1, 2); RP(0, 0, 1, 3); MA(0); RP(0, 0, 1, 4); RP(0, 0, 1, 5); MA(3); XW(1, 0); MA(2); XW(1, 1); MA(5); }
        else { MA(0); RP(0, 0, 1, 0); RP(0, 0, 1, 1); MA(3); RP(0, 0, 1, 2); RP(0, 0, 1, 3); MA(2); RP(0, 0, 1, 4); RP(0, 0, 1, 5); MA(5); XW(0, 0); MA(1); XW(0, 1); MA(4); XW(0, 2); }
        BATCH(0, 0, 1, 0, 1, XW(1, 2); XW(0, 3), XW(1, 3); XW(2, 0), XW(2, 1), XW(2, 2), XW(2, 3), NOP);
        wait_bar<1>();
        BATCH(0, 1, 0, 0, 2, XR(TK), NOP, NOP, FQ(TK, 0), FQ(TK, 2), FQ(TK, 1));
        if (nrows > 128) {
            BATCH(0, 2, 1, 1, 0, FQ(TK, 3), XR(2), FS(TK, 0), NOP, NOP, FS(TK, 1));
            BATCH(1, 0, 0, 1, 1, NOP, XW(3, 0), XW(3, 1), XW(3, 2); FQ(2, 0), XW(3, 3); FQ(2, 2), FQ(2, 1));
            wait_bar<2>();
            BATCH(1, 1, 1, 1, 2, XR(3); FQ(2, 3), FS(2, 0), NOP, FQ(3, 0), FQ(3, 2); FS(2, 1), FQ(3, 1));
            LAST(1, 2, 0, FQ(3, 3), FS(3, 0), NOP, NOP, FS(3, 1), NOP);
            half_swap<BT, H, 4, 0>(c); half_swap<BT, H, 4, 1>(c); half_swap<BT, H, 4, 2>(c); half_swap<BT, H, 4, 3>(c);
            ex_write<BT, H, 4, 2 - 2 * H>(c);
            half_swap<BT, H, 4, 4>(c); half_swap<BT, H, 4, 5>(c); half_swap<BT, H, 4, 6>(c); half_swap<BT, H, 4, 7>(c);
            ex_write<BT, H, 4, 3 - 2 * H>(c);
            lgkm_bar();
            ex_read<BT, H, 4, 2 * H>(c); ex_read<BT, H, 4, 2 * H + 1>(c);
            fin_q<BT, H, 4, 2 * H, 2 * H>(c); fin_q<BT, H, 4, 2 * H + 1, 2 * H + 1>(c);
            fin_store<BT, H, 4, H>(c);
        } else {
            LAST(0, 2, 1, FQ(TK, 3), XR(2), FS(TK, 0), FQ(2, 0), FS(TK, 1), NOP);
            XW(3, 0); XW(3, 1); XW(3, 2); XW(3, 3);
            wait_bar<2>();
            XR(3); FQ(2, 2); FQ(2, 1); FQ(2, 3); FS(2, 0); FS(2, 1);
            FQ(3, 0); FQ(3, 2); FQ(3, 1); FQ(3, 3); FS(3, 0); FS(3, 1);
        }
    }
}

__global__ __launch_bounds__(512, 2) void sparse24_gemm_kernel(const uint16_t* __restrict__ x, const uint16_t* __restrict__ w,
                                                               const uint16_t* __restrict__ bias, uint16_t* __restrict__ out) {
    __shared__ __attribute__((aligned(1024))) char smem[163840];
    LDS3 char* lds = (LDS3 char*)smem;
    const int wv = __builtin_amdgcn_readfirstlane(threadIdx.x >> 6);
    {
        uint64_t pc; unsigned o0, o1, o2, o3;
        asm volatile("s_getpc_b64 %0\n.Lsp24_base:\n\ts_mov_b32 %1, .Lsp24_path_0-.Lsp24_base\n\ts_mov_b32 %2, .Lsp24_path_1-.Lsp24_base\n\t"
                     "s_mov_b32 %3, .Lsp24_path_2-.Lsp24_base\n\ts_mov_b32 %4, .Lsp24_path_3-.Lsp24_base"
                     : "=s"(pc), "=s"(o0), "=s"(o1), "=s"(o2), "=s"(o3));
        const unsigned po = (blockIdx.x >> 3) < 28 ? (wv < 4 ? o0 : o1) : (wv < 4 ? o2 : o3);
        const char* cp = (const char*)(pc & ~(uint64_t)255) + min((int)(po & ~127u) + (int)(threadIdx.x & 255) * 32, CODE_BYTES - 128);
        asm volatile("s_mov_b32 m0, %0\n\ts_nop 0\n\tglobal_load_lds_dword %1, off" :: "s"(136 * KB + wv * 256), "v"(cp) : "memory");
    }
    const int bid = blockIdx.x, xcd = bid & 7, j = bid >> 3;
    if (j < 28) {
        const int rbl = j >> 2, cb = j & 3;
        const int r0 = xcd * 1024 + (rbl < 4 ? rbl * 160 : 640 + (rbl - 4) * 128), nr = rbl < 4 ? 160 : 128;
        const int rot = (int)((0x0630630630ull >> (4 * (rbl + cb))) & 15);
        if (wv < 4) body<false, 0>(x, w, bias, out, lds, wv, r0, nr, cb * 128, rot, 0);
        else body<false, 1>(x, w, bias, out, lds, wv - 4, r0, nr, cb * 128, rot, 0);
    } else {
        const int k = j - 28;
        const uint64_t tiles = k == 0 ? 0x0904080306050100ull : (k == 1 ? 0x120e0d0c100f0b0aull : (k == 2 ? 0x1713161119181514ull : 0x1f1b1e1a07021d1cull));
        if (wv < 4) body<true, 0>(x, w, bias, out, lds, wv, xcd * 1024, 1024, 512, (int)((0x0630u >> (4 * k)) & 15), tiles);
        else body<true, 1>(x, w, bias, out, lds, wv - 4, xcd * 1024, 1024, 512, (int)((0x0630u >> (4 * k)) & 15), tiles);
    }
}

extern "C" void kernel_launch(void* const* d_in, const int* in_sizes, int n_in, void* d_out, int out_size, void* d_ws,
                              size_t ws_size, hipStream_t stream) {
    sparse24_gemm_kernel<<<256, 512, 0, stream>>>((const uint16_t*)d_in[0], (const uint16_t*)d_in[1], (const uint16_t*)d_in[2],
                                                  (uint16_t*)d_out);
}
```
